```python
import torch

def _softmax(x, axis=-1):
    return torch.softmax(x, dim=axis)
def _gelu(x):
    return torch.nn.functional.gelu(x, approximate="tanh")
def _silu(x):
    return torch.nn.functional.silu(x)
def _sigmoid(x):
    return torch.sigmoid(x)
def _layernorm(x, axis=-1, eps=1e-5):
    m = x.mean(dim=axis, keepdim=True)
    v = x.var(dim=axis, keepdim=True, unbiased=False)
    return (x - m) / torch.sqrt(v + eps)
def _scatter_add(tgt, idx, src, axis=0):
    return tgt.clone().index_add_(axis, idx, src)
def _segment_sum(src, seg, n):
    out = torch.zeros((n,) + src.shape[1:], dtype=src.dtype)
    return out.index_add_(0, seg, src)
def _pad(x, pw):
    flat = tuple(v for pair in reversed(pw) for v in pair)
    return torch.nn.functional.pad(x, flat)

kernel_name = 'recurrent_scan__linear_kernel_hedgehog__geglu_ffn__prompt'

def setup_inputs():

    _w = (lambda a: (lambda t: t.to(torch.bfloat16) if t.is_floating_point() else t)(torch.as_tensor(a)))
    def _A_setup():
        B, S, D, H = 1, 2048, 512, 32
        Dh = D // H
        return {
            "x": _w((lambda *s: torch.randn(*s, dtype=torch.bfloat16) * 0.05)(B, S, D)),
            "wqkv": _w((lambda *s: torch.randn(*s, dtype=torch.bfloat16) * 0.2)(D, 3 * D)),
            "w_phi": _w((lambda *s: torch.randn(*s, dtype=torch.bfloat16) * 0.2)(H, Dh, Dh) * (Dh ** -0.5)),
            "wo": _w((lambda *s: torch.randn(*s, dtype=torch.bfloat16) * 0.05)(D, D)),
        }
    _A_in = _A_setup()

    def _B_setup():
        B, S, D, F = 1, 2048, 512, 1376
        return {
            "x": _w((lambda *s: torch.randn(*s, dtype=torch.bfloat16) * 0.05)(B, S, D)),
            "w_gate": _w((lambda *s: torch.randn(*s, dtype=torch.bfloat16) * 0.05)(D, F)),
            "w_up": _w((lambda *s: torch.randn(*s, dtype=torch.bfloat16) * 0.05)(D, F)),
            "w_down": _w((lambda *s: torch.randn(*s, dtype=torch.bfloat16) * 0.05)(F, D)),
        }
    _B_in = _B_setup()

    return {
        "A_x": _A_in["x"],
        "A_wqkv": _A_in["wqkv"],
        "A_w_phi": _A_in["w_phi"],
        "A_wo": _A_in["wo"],
        "B_x": _B_in["x"],
        "B_w_gate": _B_in["w_gate"],
        "B_w_up": _B_in["w_up"],
        "B_w_down": _B_in["w_down"],
    }

def reference(A_x, A_wqkv, A_w_phi, A_wo, B_x, B_w_gate, B_w_up, B_w_down):
    def _A_ref(x, wqkv, w_phi, wo):
        B, S, D = x.shape
        H = 32; Dh = D // H
        qkv = (lambda x, *d: x.permute(*d))((x @ wqkv).reshape(B, S, 3, H, Dh), 2, 0, 3, 1, 4)
        q, k, v = qkv[0], qkv[1], qkv[2]
        pq = torch.einsum("bhsd,hdf->bhsf", q, w_phi)
        pk = torch.einsum("bhsd,hdf->bhsf", k, w_phi)
        phi_q = torch.concatenate([_softmax(pq, axis=-1), _softmax(-pq, axis=-1)], axis=-1)
        phi_k = torch.concatenate([_softmax(pk, axis=-1), _softmax(-pk, axis=-1)], axis=-1)
        kv = torch.einsum("bhsf,bhsd->bhfd", phi_k, v)
        z  = torch.einsum("bhsf->bhf", phi_k)
        num = torch.einsum("bhqf,bhfd->bhqd", phi_q, kv)
        den = torch.einsum("bhqf,bhf->bhq", phi_q, z)[..., None] + 1e-6
        o = num / den
        return (lambda x, *d: x.permute(*d))(o, 0, 2, 1, 3).reshape(B, S, D) @ wo

    def _B_ref(x, w_gate, w_up, w_down):
        return (_gelu(x @ w_gate) * (x @ w_up)) @ w_down

    x = A_x
    xn = _layernorm(x, axis=-1)
    s = _A_ref(xn, A_wqkv, A_w_phi, A_wo)
    g = _silu(_B_ref(xn, B_w_gate, B_w_up, B_w_down))
    return x + s * g
```

```cpp
#include <hip/hip_runtime.h>
#include <hip/hip_bf16.h>
typedef unsigned short u16;
__device__ __forceinline__ float bf2f(u16 v){ return __uint_as_float(((unsigned)v)<<16); }
__device__ __forceinline__ u16 f2bf(float f){ __hip_bfloat16 b = __float2bfloat16(f); return *(u16*)&b; }

__global__ void k_ln(const u16* x, u16* xn){
  int row = blockIdx.x; int t = threadIdx.x;
  float v[8]; float s=0;
  for(int i=0;i<8;i++){ v[i]=bf2f(x[row*512+t*8+i]); s+=v[i]; }
  for(int m=32;m>=1;m>>=1) s+=__shfl_xor(s,m);
  float mean=s/512.f; float q=0;
  for(int i=0;i<8;i++){ v[i]-=mean; q+=v[i]*v[i]; }
  for(int m=32;m>=1;m>>=1) q+=__shfl_xor(q,m);
  float r=rsqrtf(q/512.f+1e-5f);
  for(int i=0;i<8;i++) xn[row*512+t*8+i]=f2bf(v[i]*r);
}
__global__ void k_gemm(const u16* A,const u16* B,float* C,int M,int N,int K){
  __shared__ float As[16][17], Bs[16][17];
  int tx=threadIdx.x, ty=threadIdx.y; int row=blockIdx.y*16+ty, col=blockIdx.x*16+tx;
  float acc=0;
  for(int k0=0;k0<K;k0+=16){
    As[ty][tx]=bf2f(A[(size_t)row*K+k0+tx]);
    Bs[ty][tx]=bf2f(B[(size_t)(k0+ty)*N+col]);
    __syncthreads();
    for(int k=0;k<16;k++) acc+=As[ty][k]*Bs[k][tx];
    __syncthreads();
  }
  C[(size_t)row*N+col]=acc;
}
__global__ void k_phi(const float* qkv,const u16* wphi,float* phi  ){
  int idx=blockIdx.x*blockDim.x+threadIdx.x;
  int h=idx&31, t=(idx>>5)&2047, w=idx>>16;
  float q[16]; for(int d=0;d<16;d++) q[d]=bf2f(f2bf(qkv[(size_t)t*1536+w*512+h*16+d]));
  float p[16]; float mx=-1e30f, mn=1e30f;
  for(int f=0;f<16;f++){ float a=0; for(int d=0;d<16;d++) a+=q[d]*bf2f(wphi[h*256+d*16+f]); p[f]=a; mx=fmaxf(mx,a); mn=fminf(mn,a);}
  float s1=0,s2=0; float e1[16],e2[16];
  for(int f=0;f<16;f++){ e1[f]=__expf(p[f]-mx); e2[f]=__expf(mn-p[f]); s1+=e1[f]; s2+=e2[f]; }
  float* o=phi+(size_t)idx*32;
  for(int f=0;f<16;f++){ o[f]=e1[f]/s1; o[16+f]=e2[f]/s2; }
}
__global__ void k_kv(const float* qkv,const float* phik,float* kv  ){
  int h=blockIdx.x; int f=threadIdx.x&31, d=threadIdx.x>>5;
  float a=0;
  for(int t=0;t<2048;t++){ float v = d<16? qkv[(size_t)t*1536+1024+h*16+d] : 1.f; a+=phik[((size_t)t*32+h)*32+f]*v; }
  kv[(h*32+f)*17+d]=a;
}
__global__ void k_o(const float* phiq,const float* kv,u16* o){
  int idx=blockIdx.x*blockDim.x+threadIdx.x;
  int d=idx&15,h=(idx>>4)&31,t=idx>>9;
  float n=0,dn=0;
  for(int f=0;f<32;f++){ float p=phiq[((size_t)t*32+h)*32+f]; n+=p*kv[(h*32+f)*17+d]; dn+=p*kv[(h*32+f)*17+16]; }
  o[idx]=f2bf(n/(dn+1e-6f));
}
__global__ void k_h(const float* g,const float* u,u16* h,int n){
  int i=blockIdx.x*blockDim.x+threadIdx.x; if(i>=n) return;
  float x=g[i]; float t=tanhf(0.7978845608f*(x+0.044715f*x*x*x));
  h[i]=f2bf(0.5f*x*(1.f+t)*u[i]);
}
__global__ void k_fin(const u16* x,const float* s,const float* d,u16* out){
  int i=blockIdx.x*blockDim.x+threadIdx.x;
  float dd=d[i]; float g=dd/(1.f+__expf(-dd));
  out[i]=f2bf(bf2f(x[i])+s[i]*g);
}
extern "C" void kernel_launch(void* const* d_in, const int* in_sizes, int n_in,
                              void* d_out, int out_size, void* d_ws, size_t ws_size,
                              hipStream_t stream) {
  const u16* x=(const u16*)d_in[0]; const u16* wqkv=(const u16*)d_in[1]; const u16* wphi=(const u16*)d_in[2]; const u16* wo=(const u16*)d_in[3];
  const u16* wg=(const u16*)d_in[5]; const u16* wu=(const u16*)d_in[6]; const u16* wd=(const u16*)d_in[7];
  char* ws=(char*)d_ws; size_t off=0;
  auto alloc=[&](size_t b){ void* p=ws+off; off+=(b+255)&~(size_t)255; return p; };
  u16* xn=(u16*)alloc(2048*512*2); float* qkv=(float*)alloc(2048*1536*4); float* phi=(float*)alloc(2ull*2048*32*32*4);
  float* kv=(float*)alloc(32*32*17*4); u16* o=(u16*)alloc(2048*512*2); float* g=(float*)alloc(2048*1376*4); float* u=(float*)alloc(2048*1376*4);
  u16* h=(u16*)alloc(2048*1376*2); float* s=(float*)alloc(2048*512*4); float* d=(float*)alloc(2048*512*4);
  k_ln<<<2048,64,0,stream>>>(x,xn);
  k_gemm<<<dim3(1536/16,128),dim3(16,16),0,stream>>>(xn,wqkv,qkv,2048,1536,512);
  k_phi<<<2*2048*32/256,256,0,stream>>>(qkv,wphi,phi);
  k_kv<<<32,32*17,0,stream>>>(qkv,phi+(size_t)2048*32*32,kv);
  k_o<<<2048*512/256,256,0,stream>>>(phi,kv,o);
  k_gemm<<<dim3(512/16,128),dim3(16,16),0,stream>>>(o,wo,s,2048,512,512);
  k_gemm<<<dim3(1376/16,128),dim3(16,16),0,stream>>>(xn,wg,g,2048,1376,512);
  k_gemm<<<dim3(1376/16,128),dim3(16,16),0,stream>>>(xn,wu,u,2048,1376,512);
  k_h<<<2048*1376/256,256,0,stream>>>(g,u,h,2048*1376);
  k_gemm<<<dim3(512/16,128),dim3(16,16),0,stream>>>(h,wd,d,2048,512,1376);
  k_fin<<<2048*512/256,256,0,stream>>>(x,s,d,(u16*)d_out);
}
```

```cpp
#include <hip/hip_runtime.h>
#include <hip/hip_bf16.h>

typedef unsigned short u16;
typedef unsigned int u32;
typedef unsigned long long u64;
typedef short s16x4 __attribute__((ext_vector_type(4)));
typedef short s16x8 __attribute__((ext_vector_type(8)));
typedef __bf16 bf16x8 __attribute__((ext_vector_type(8)));
typedef __bf16 bf16x2 __attribute__((ext_vector_type(2)));
typedef float f32x2 __attribute__((ext_vector_type(2)));
typedef float f32x4 __attribute__((ext_vector_type(4)));
typedef float f32x16 __attribute__((ext_vector_type(16)));
typedef u32 u32x2 __attribute__((ext_vector_type(2)));
typedef u32 u32x4 __attribute__((ext_vector_type(4)));
#define LAS __attribute__((address_space(3)))
#define DI __device__ __forceinline__
#define MFMA32(a, b, c) __builtin_amdgcn_mfma_f32_32x32x16_bf16((a), (b), (c), 0, 0, 0)

#define STATMODE 0
#define MAP1 0
#define MAP2 0

constexpr int SEQ = 2048, DM = 512, FF = 1376, NH = 32;
constexpr int NPAIR = 86;
constexpr size_t WS_H = 0;
constexpr size_t WS_PQ = WS_H + (size_t)64 * 88 * 1024;
constexpr size_t WS_KVP = WS_PQ + (size_t)64 * 32 * 2048;
constexpr size_t WS_KVF = WS_KVP + (size_t)16 * 32 * 4096;
constexpr size_t WS_END = WS_KVF + 65536;

#define STAMP(ws, s) do {} while (0)

DI void dma16(const void* sbase, u32 voff, u32 lds) {
  asm volatile("s_mov_b32 m0, %2\n\ts_nop 0\n\tglobal_load_lds_dwordx4 %0, %1" ::"v"(voff), "s"(sbase), "s"(lds) : "memory");
}
#define WAITBAR(n) asm volatile("s_waitcnt vmcnt(" #n ")\n\ts_barrier" ::: "memory")
#define SCHED0 __builtin_amdgcn_sched_barrier(0)

DI bf16x8 tr_frag(LAS char* p0, LAS char* p1) {
  s16x4 a = __builtin_amdgcn_ds_read_tr16_b64_v4i16((LAS s16x4*)p0);
  s16x4 b = __builtin_amdgcn_ds_read_tr16_b64_v4i16((LAS s16x4*)p1);
  return __builtin_bit_cast(bf16x8, __builtin_shufflevector(a, b, 0, 1, 2, 3, 4, 5, 6, 7));
}
DI u32 pk2(float a, float b) {
  f32x2 v = {a, b};
  return __builtin_bit_cast(u32, __builtin_convertvector(v, bf16x2));
}
DI float bfr(float a) { return __uint_as_float(pk2(a, a) & 0xffff0000u); }
DI float lo_f(u32 w) { return __uint_as_float(w << 16); }
DI float hi_f(u32 w) { return __uint_as_float(w & 0xffff0000u); }
DI float xhalf(float v) {
  return __shfl_xor(v, 32);
}
DI float fexp2(float x) { return __builtin_amdgcn_exp2f(x); }
DI float frcp(float x) { return __builtin_amdgcn_rcpf(x); }

DI void sum8(const bf16x8& f, float& s) {
  u32x4 w = __builtin_bit_cast(u32x4, f);
#pragma unroll
  for (int i = 0; i < 4; i++) { s += lo_f(w[i]); s += hi_f(w[i]); }
}
DI void sum8sq(const bf16x8& f, float& s, float& q) {
  u32x4 w = __builtin_bit_cast(u32x4, f);
#pragma unroll
  for (int i = 0; i < 4; i++) { float a = lo_f(w[i]), b = hi_f(w[i]); s += a; q = fmaf(a, a, q); s += b; q = fmaf(b, b, q); }
}

constexpr int SEG1 = 9344, WSLOT = 18688, XRING = 6 * WSLOT, XSLOT = 16384, STAT = XRING + 3 * XSLOT;
constexpr int RSTAT = STAT + 1152, LDS1 = RSTAT + 1024;
constexpr int WPHI_L = XRING + 2 * XSLOT;
constexpr int PHIK_L = 0, VIMG_L = 65536, ONES_L = VIMG_L + 32768;

template <bool B> struct BoolC { static constexpr bool value = B; };
struct K1Ctx {
  const u16* seg0; const u16* seg1; const u16* xt; const u16* wphi;
  int ld, np, type;
};

template <bool NORM>
DI void dual_softmax(const f32x16& p, int o, u32x4& fp, u32x4& fm) {
  const float L2E = 1.4426950408889634f;
  float mx = fmaxf(fmaxf(fmaxf(p[o], p[o + 1]), fmaxf(p[o + 2], p[o + 3])), fmaxf(fmaxf(p[o + 4], p[o + 5]), fmaxf(p[o + 6], p[o + 7])));
  float mn = fminf(fminf(fminf(p[o], p[o + 1]), fminf(p[o + 2], p[o + 3])), fminf(fminf(p[o + 4], p[o + 5]), fminf(p[o + 6], p[o + 7])));
  mx = fmaxf(mx, xhalf(mx));
  mn = fminf(mn, xhalf(mn));
  const float cx = -mx * L2E, cn = mn * L2E;
  float ep[8], em[8], sp = 0.f, sm = 0.f;
#pragma unroll
  for (int j = 0; j < 8; j++) {
    ep[j] = fexp2(fmaf(p[o + j], L2E, cx));
    em[j] = fexp2(fmaf(p[o + j], -L2E, cn));
    sp += ep[j]; sm += em[j];
  }
  sp += xhalf(sp);
  sm += xhalf(sm);
  float rm = frcp(sm);
  if (NORM) {
    const float rp = frcp(sp);
#pragma unroll
    for (int j = 0; j < 8; j++) ep[j] *= rp;
  } else {
    rm *= sp;
  }
#pragma unroll
  for (int j = 0; j < 8; j++) em[j] *= rm;
#pragma unroll
  for (int j = 0; j < 4; j++) { fp[j] = pk2(ep[2 * j], ep[2 * j + 1]); fm[j] = pk2(em[2 * j], em[2 * j + 1]); }
}

template <int NT>
DI void k1_wave(LAS char* sm, const K1Ctx& c, char* ws, int ti, int jcol, int p0, int w, int lane) {
  const int tg = w & 3, pg = w >> 2;
  const int r = lane & 31, h = lane >> 5, g16 = (lane >> 4) & 1, q4 = (lane & 15) >> 2, pp = lane & 3;
  const int T0 = pg ? (c.np == 9 ? 5 : 4) : 0;
  const bool wrole = w < 6;
  u32 voff[3];
  const char* sbase;
  u32 ldst;
  if (wrole) {
    const int seg = w >= 3, m0 = 3 * (w - 3 * seg), emax = 2 * c.np - 1;
#pragma unroll
    for (int m = 0; m < 3; m++) {
      const int ch = 64 * (m0 + m) + lane, row = ch / 18;
      int e = ch - row * 18; e = e > emax ? emax : e;
      voff[m] = (u32)(row * c.ld + 8 * e) * 2u;
    }
    sbase = (const char*)(seg ? c.seg1 : c.seg0);
    ldst = seg * SEG1 + m0 * 1024;
  } else {
    const int rr = lane >> 3, pos = lane & 7, f = lane >> 4;
    voff[0] = rr * 1024 + 16 * (pos ^ f);
    voff[1] = (rr + 8) * 1024 + 16 * (pos ^ f ^ 4);
    voff[2] = lane * 16;
    sbase = (const char*)c.xt + (w - 6) * 65536;
    ldst = XRING + (w - 6) * 8192;
  }
  const u32 wstep = 64u * (u32)c.ld;
  auto issue_w = [&](int u, u32 slot) {
    const char* b = sbase + (size_t)u * wstep;
#pragma unroll
    for (int m = 0; m < 3; m++) dma16(b, voff[m], ldst + slot + m * 1024);
  };
  auto issue_x = [&](int s, u32 slot) {
    const char* b = sbase + s * 128;
#pragma unroll
    for (int m = 0; m < 4; m++) {
      dma16(b + m * 16384, voff[0], ldst + slot + m * 2048);
      dma16(b + m * 16384, voff[1], ldst + slot + m * 2048 + 1024);
    }
  };
  if (wrole) {
#pragma unroll
    for (int u = 0; u < 6; u++) issue_w(u, u * WSLOT);
  } else {
#pragma unroll
    for (int s = 0; s < 3; s++) issue_x(s, s * XSLOT);
  }
  u32 wa[NT];
#pragma unroll
  for (int s = 0; s < NT; s++) {
    int t = s + tg; t = t >= NT ? t - NT : t;
    wa[s] = g16 * SEG1 + (8 * h + q4) * 288 + 8 * pp + 32 * (T0 + t);
  }
  const u32 xa = XRING + (32 * tg + r) * 128 + 16 * (h ^ ((r >> 1) & 7));
  f32x16 acc[NT];
#pragma unroll
  for (int s = 0; s < NT; s++)
#pragma unroll
    for (int j = 0; j < 16; j++) acc[s][j] = 0.f;
  float cs0 = 0.f, cs1 = 0.f, s1 = 0.f, s2 = 0.f;
  bf16x8 fa[2][NT], fb[2];
  auto rd = [&](int buf, u32 wslot, int ks, u32 xslot, int kk) {
    fb[buf] = *(LAS bf16x8*)(sm + ((xa ^ (kk << 5)) + xslot));
#pragma unroll
    for (int s = 0; s < NT; s++) {
      LAS char* p = sm + (wa[s] + wslot) + ks * 4608;
      fa[buf][s] = tr_frag(p, p + 1152);
    }
  };
  STAMP(ws, 1);
  if (wrole) WAITBAR(15); else WAITBAR(16);
  STAMP(ws, 2);
  auto mainloop = [&](auto xs_tag) {
  constexpr bool XS = decltype(xs_tag)::value;
  auto mm = [&](int buf) {
#pragma unroll
    for (int s = 0; s < NT; s++) acc[s] = MFMA32(fa[buf][s], fb[buf], acc[s]);
    sum8(fa[buf][0], cs0);
    if (NT == 5) sum8(fa[buf][1], cs1);
    if (XS) sum8sq(fb[buf], s1, s2);
  };
  rd(0, 0, 0, 0, 0);
  u32 wcur = 0, xcur = 0;
#pragma unroll 1
  for (int s = 0; s < 8; s++) {
    {
      const int u = 2 * s + 1;
      const u32 wnext = wcur + WSLOT;
      rd(1, wcur, 1, xcur, 1);
      mm(0);
      SCHED0;
      if (wrole) {
        if (u == 1) WAITBAR(12); else if (u <= 11) WAITBAR(9); else if (u == 13) WAITBAR(6); else WAITBAR(0);
        if (u >= 3 && u <= 11) issue_w(u + 4, wcur == 0 ? 5 * WSLOT : wcur - WSLOT);
      } else {
        asm volatile("s_barrier" ::: "memory");
        if (u >= 3 && u <= 11) issue_x(s + 2, xcur == 0 ? 2 * XSLOT : xcur - XSLOT);
        else if (u == 13 && c.type) {
          const char* b = (const char*)c.wphi + (w - 6) * (c.type == 1 ? 4096 : 2048);
#pragma unroll
          for (int m = 0; m < 4; m++) dma16(b + (c.type == 1 ? m : (m & 1)) * 1024, voff[2], WPHI_L + (w - 6) * (c.type == 1 ? 4096 : 2048) + (c.type == 1 ? m : (m & 1)) * 1024);
        }
      }
      rd(0, wnext, 0, xcur, 2);
      mm(1);
      wcur = wnext;
    }
    {
      const int u = 2 * s + 2;
      const u32 wnext = wcur == 5 * WSLOT ? 0 : wcur + WSLOT;
      const u32 xnext = xcur == 2 * XSLOT ? 0 : xcur + XSLOT;
      rd(1, wcur, 1, xcur, 3);
      mm(0);
      SCHED0;
      if (wrole) {
        if (u <= 12) WAITBAR(9); else if (u == 14) WAITBAR(3); else WAITBAR(0);
        if (u <= 10) issue_w(u + 4, wcur - WSLOT);
      } else {
        if (u <= 12) WAITBAR(8); else if (u == 14 && c.type) WAITBAR(4); else WAITBAR(0);
      }
      rd(0, wnext, 0, xnext, 0);
      mm(1);
      wcur = wnext; xcur = xnext;
    }
  }
  };
  if (NT == 4 && pg) mainloop(BoolC<true>{}); else mainloop(BoolC<false>{});
  STAMP(ws, 3);
  {
    LAS float* cs = (LAS float*)(sm + STAT);
    cs0 += xhalf(cs0);
    if (h == 0) cs[(T0 + tg) * 32 + r] = cs0;
    if (NT == 5) { cs1 += xhalf(cs1); if (h == 0 && tg == 3) cs[4 * 32 + r] = cs1; }
    if (pg) {
      s1 += xhalf(s1); s2 += xhalf(s2);
      const float mean = s1 * (1.f / 512.f);
      const float var = fmaxf(s2 * (1.f / 512.f) - mean * mean, 0.f);
      const float sd = bfr(__builtin_sqrtf(bfr(bfr(var) + 1e-5f)));
      const float a = 1.f / sd;
      if (h == 0) *(LAS f32x2*)(sm + RSTAT + (32 * tg + r) * 8) = (f32x2){a, -bfr(mean) * a};
    }
  }
  __syncthreads();
  const f32x2 rs = *(LAS f32x2*)(sm + RSTAT + (32 * tg + r) * 8);
#pragma unroll
  for (int s = 0; s < NT; s++) {
    int t = s + tg; t = t >= NT ? t - NT : t;
    LAS f32x4* cp = (LAS f32x4*)(sm + STAT + ((T0 + t) * 32 + 4 * h) * 4);
#pragma unroll
    for (int i = 0; i < 4; i++) {
      const f32x4 cv = cp[2 * i];
#pragma unroll
      for (int j = 0; j < 4; j++) acc[s][4 * i + j] = fmaf(acc[s][4 * i + j], rs[0], rs[1] * cv[j]);
    }
  }
  const int tg32 = 4 * ti + tg;
  if (c.type == 0) {
    const float K0 = -2.f * 0.7978845608028654f * 1.4426950408889634f, K1c = K0 * 0.044715f;
#pragma unroll
    for (int s = 0; s < NT; s++) {
      int t = s + tg; t = t >= NT ? t - NT : t;
      float hv[8];
#pragma unroll
      for (int j = 0; j < 8; j++) {
        const float g = acc[s][j], u = acc[s][8 + j];
        const float e = fexp2(g * fmaf(g * g, K1c, K0));
        hv[j] = g * u * frcp(1.f + e);
      }
      u32x4 o = {pk2(hv[0], hv[1]), pk2(hv[2], hv[3]), pk2(hv[4], hv[5]), pk2(hv[6], hv[7])};
      *(u32x4*)(ws + WS_H + ((size_t)(tg32 * NPAIR + p0 + T0 + t) * 64 + lane) * 16) = o;
    }
  } else if (NT == 4) {
    const u32 wpa = WPHI_L + (4 * h + q4) * 32 + 8 * pp;
    f32x16 zero;
#pragma unroll
    for (int j = 0; j < 16; j++) zero[j] = 0.f;
    if (c.type == 1) {
#pragma unroll
      for (int s = 0; s < 4; s++) {
        const int t = T0 + ((s + tg) & 3);
        LAS char* p = sm + wpa + (t + 8 * g16) * 512;
        const u32x4 wab = __builtin_bit_cast(u32x4, tr_frag(p, p + 256));
        u32x4 wA, wB, qa, qb;
#pragma unroll
        for (int j = 0; j < 4; j++) {
          wA[j] = g16 ? 0u : wab[j]; wB[j] = g16 ? wab[j] : 0u;
          qa[j] = pk2(acc[s][2 * j], acc[s][2 * j + 1]); qb[j] = pk2(acc[s][8 + 2 * j], acc[s][9 + 2 * j]);
        }
        f32x16 P = MFMA32(__builtin_bit_cast(bf16x8, wA), __builtin_bit_cast(bf16x8, qa), zero);
        P = MFMA32(__builtin_bit_cast(bf16x8, wB), __builtin_bit_cast(bf16x8, qb), P);
        u32x4 fp, fm;
        char* dst = ws + WS_PQ + ((size_t)(tg32 * NH + 16 * (jcol - 10) + t) * 128 + lane) * 16;
        dual_softmax<false>(P, 0, fp, fm);
        *(u32x4*)dst = fp; *(u32x4*)(dst + 1024) = fm;
        dual_softmax<false>(P, 8, fp, fm);
        *(u32x4*)(dst + 8 * 2048) = fp; *(u32x4*)(dst + 8 * 2048 + 1024) = fm;
      }
    } else {
      const int tok = 32 * tg + r;
      const u32 pw = PHIK_L + tok * 64, vw = VIMG_L + tok * 32;
      const int fsw = (r >> 1) & 7, vsw = (r >> 2) & 3;
      if (w == 0 && lane == 0) *(LAS u32x2*)(sm + ONES_L) = (u32x2){0x3f803f80u, 0x3f803f80u};
#pragma unroll
      for (int s = 0; s < 4; s += 2) {
        const int ta = T0 + ((s + tg) & 3), tb = T0 + ((s + 1 + tg) & 3);
        LAS char* p = sm + wpa + (g16 ? tb : ta) * 512;
        const u32x4 wab = __builtin_bit_cast(u32x4, tr_frag(p, p + 256));
        u32x4 wA, wB, ka, kb;
#pragma unroll
        for (int j = 0; j < 4; j++) {
          wA[j] = g16 ? 0u : wab[j]; wB[j] = g16 ? wab[j] : 0u;
          ka[j] = pk2(acc[s][2 * j], acc[s][2 * j + 1]); kb[j] = pk2(acc[s + 1][2 * j], acc[s + 1][2 * j + 1]);
        }
        f32x16 P = MFMA32(__builtin_bit_cast(bf16x8, wA), __builtin_bit_cast(bf16x8, ka), zero);
        P = MFMA32(__builtin_bit_cast(bf16x8, wB), __builtin_bit_cast(bf16x8, kb), P);
#pragma unroll
        for (int e = 0; e < 2; e++) {
          const int t = e ? tb : ta;
          u32x4 fp, fm;
          dual_softmax<true>(P, 8 * e, fp, fm);
          LAS char* pd = sm + pw + t * 8192;
          *(LAS u32x2*)(pd + 8 * ((0 + h) ^ fsw)) = (u32x2){fp[0], fp[1]};
          *(LAS u32x2*)(pd + 8 * ((2 + h) ^ fsw)) = (u32x2){fp[2], fp[3]};
          *(LAS u32x2*)(pd + 8 * ((4 + h) ^ fsw)) = (u32x2){fm[0], fm[1]};
          *(LAS u32x2*)(pd + 8 * ((6 + h) ^ fsw)) = (u32x2){fm[2], fm[3]};
          LAS char* vd = sm + vw + t * 4096;
          const f32x16& a = acc[s + e];
          *(LAS u32x2*)(vd + 8 * ((0 + h) ^ vsw)) = (u32x2){pk2(a[8], a[9]), pk2(a[10], a[11])};
          *(LAS u32x2*)(vd + 8 * ((2 + h) ^ vsw)) = (u32x2){pk2(a[12], a[13]), pk2(a[14], a[15])};
        }
      }
      __syncthreads();
      f32x16 kv = zero;
#pragma unroll
      for (int ks = 0; ks < 8; ks++) {
        const int t0 = 16 * ks + 8 * h + q4, t1 = t0 + 4;
        LAS char* pa0 = sm + PHIK_L + w * 8192 + t0 * 64 + 8 * ((4 * g16 + pp) ^ ((t0 >> 1) & 7));
        LAS char* pa1 = sm + PHIK_L + w * 8192 + t1 * 64 + 8 * ((4 * g16 + pp) ^ ((t1 >> 1) & 7));
        LAS char* pb0 = sm + (g16 ? ONES_L : VIMG_L + w * 4096 + t0 * 32 + 8 * (pp ^ ((t0 >> 2) & 3)));
        LAS char* pb1 = sm + (g16 ? ONES_L : VIMG_L + w * 4096 + t1 * 32 + 8 * (pp ^ ((t1 >> 2) & 3)));
        kv = MFMA32(tr_frag(pa0, pa1), tr_frag(pb0, pb1), kv);
      }
      if (r <= 16) {
        char* dst = ws + WS_KVP + (size_t)(ti * NH + 8 * (jcol - 12) + w) * 4096 + lane * 16;
#pragma unroll
        for (int g = 0; g < 4; g++) *(f32x4*)(dst + g * 1024) = (f32x4){kv[4 * g], kv[4 * g + 1], kv[4 * g + 2], kv[4 * g + 3]};
      }
    }
  }
}

__global__ __launch_bounds__(512, 2) void k1(const u16* __restrict__ x, const u16* __restrict__ wqkv, const u16* __restrict__ wphi,
                                             const u16* __restrict__ wg, const u16* __restrict__ wu, char* __restrict__ ws) {
  __shared__ __attribute__((aligned(256))) char smem[LDS1];
  LAS char* sm = (LAS char*)smem;
  STAMP(ws, 0);
  const int b = blockIdx.x, xcd = b & 7, loc = b >> 3;
  const int ti = loc & 15, jc = 2 * xcd + (loc >> 4);
  const int w = __builtin_amdgcn_readfirstlane(threadIdx.x >> 6), lane = threadIdx.x & 63;
  K1Ctx c;
  int p0 = 0;
  c.xt = x + (size_t)ti * 128 * DM;
  c.wphi = wphi;
  if (jc < 10) {
    c.type = 0; c.ld = FF; c.np = jc < 6 ? 9 : 8;
    p0 = jc < 6 ? 9 * jc : 54 + 8 * (jc - 6);
    c.seg0 = wg + 16 * p0; c.seg1 = wu + 16 * p0;
  } else if (jc < 12) {
    c.type = 1; c.ld = 3 * DM; c.np = 8;
    c.seg0 = wqkv + 256 * (jc - 10); c.seg1 = c.seg0 + 128;
    c.wphi = wphi + (jc - 10) * 16 * 256;
  } else {
    c.type = 2; c.ld = 3 * DM; c.np = 8;
    c.seg0 = wqkv + DM + 128 * (jc - 12); c.seg1 = c.seg0 + DM;
    c.wphi = wphi + (jc - 12) * 8 * 256;
  }
  if (c.np == 9 && w < 4) k1_wave<5>(sm, c, ws, ti, jc, p0, w, lane);
  else k1_wave<4>(sm, c, ws, ti, jc, p0, w, lane);
  STAMP(ws, 4);
}

__global__ __launch_bounds__(64) void kred(char* __restrict__ ws) {
  const int head = blockIdx.x >> 2, g = blockIdx.x & 3, lane = threadIdx.x;
  const int r = lane & 31, l2 = r > 16 ? (lane & 32) + 16 : lane;
  const char* src = ws + WS_KVP + (size_t)head * 4096 + g * 1024 + l2 * 16;
  f32x4 v[16];
#pragma unroll
  for (int i = 0; i < 16; i++) v[i] = *(const f32x4*)(src + (size_t)i * NH * 4096);
  f32x4 s = v[0];
#pragma unroll
  for (int i = 1; i < 16; i++) s += v[i];
  if (r >= 16 && g < 2) s += 1e-6f;
  *(u32x2*)(ws + WS_KVF + head * 2048 + (g >> 1) * 1024 + lane * 16 + (g & 1) * 8) = (u32x2){pk2(s[0], s[1]), pk2(s[2], s[3])};
}

constexpr int SLOT2 = 16384, NSLOT2 = 9, LDS2 = NSLOT2 * SLOT2, NST2 = 38;

__global__ __launch_bounds__(512, 2) void k2(const u16* __restrict__ x, const u16* __restrict__ wo, const u16* __restrict__ wd,
                                             char* __restrict__ ws, u16* __restrict__ out) {
  __shared__ __attribute__((aligned(256))) char smem[LDS2];
  LAS char* sm = (LAS char*)smem;
  STAMP(ws, 8);
  const int b = blockIdx.x, xcd = b & 7, loc = b >> 3;
  const int ti = 4 * xcd + (loc & 3), cj = loc >> 2;
  const int w = __builtin_amdgcn_readfirstlane(threadIdx.x >> 6), lane = threadIdx.x & 63;
  const int tg = w & 1, cg = (w >> 1) & 1, kh = w >> 2;
  const int r = lane & 31, h = lane >> 5, g16 = (lane >> 4) & 1, q4 = (lane & 15) >> 2, pp = lane & 3;
  const u32 vlin = lane * 16;
  const u32 vw = (lane >> 3) * 1024 + 16 * ((lane & 7) ^ (((lane >> 4) & 1) << 2));
  const char* hsrc = ws + WS_H + (size_t)((2 * ti + (w >> 2)) * NPAIR + (w & 3)) * 1024;
  const char* dsrc = (const char*)wd + (size_t)(8 * w) * 1024 + cj * 128;
  const char* psrc = ws + WS_PQ + (size_t)((2 * ti + (w >> 2)) * NH) * 2048 + (w & 3) * 1024;
  const char* osrc = w < 4 ? (const char*)wo + (size_t)(8 * w) * 1024 + cj * 128 : ws + WS_KVF + (w - 4) * 1024;
  const u32 vo = w < 4 ? vw : vlin;
  const u32 ostep = w < 4 ? 32768u : 4096u;
  auto issue = [&](int t, u32 slot) {
    if (t < 22) {
      const int tt = (t == 21 && w >= 4) ? 20 : t;
      dma16(hsrc + t * 4096, vlin, slot + w * 1024);
      dma16(dsrc + (size_t)tt * 65536, vw, slot + 8192 + w * 1024);
    } else {
      const int u = t - 22;
      dma16(psrc + u * 4096, vlin, slot + w * 1024);
      dma16(osrc + (size_t)u * ostep, vo, slot + 8192 + w * 1024);
    }
  };
#pragma unroll
  for (int t = 0; t < 8; t++) issue(t, t * SLOT2);
  const size_t orow = (size_t)(64 * ti + 32 * tg + r) * DM + 64 * cj + 32 * cg + 16 * kh;
  const u32x2 xr0 = *(const u32x2*)(x + orow + 4 * h), xr1 = *(const u32x2*)(x + orow + 8 + 4 * h);
  f32x16 accD, accS, zero;
#pragma unroll
  for (int j = 0; j < 16; j++) { accD[j] = 0.f; accS[j] = 0.f; zero[j] = 0.f; }
  const u32 wa = 8192 + (4 * h + q4) * 128 + ((cg ^ (q4 >> 1)) << 6) + 32 * g16 + 8 * pp;
  const u32 ha = tg * 4096 + lane * 16;
  u32 slot = 0;
  STAMP(ws, 9);
#pragma unroll 1
  for (int t = 0; t < 22; t++) {
    SCHED0;
    asm volatile("s_waitcnt vmcnt(14) lgkmcnt(0)\n\ts_barrier" ::: "memory");
    issue(t + 8, slot == 0 ? (NSLOT2 - 1) * SLOT2 : slot - SLOT2);
    LAS char* pw = sm + slot + wa + kh * 2048;
    LAS char* ph = sm + slot + ha + kh * 1024;
    const bf16x8 a0 = tr_frag(pw, pw + 1024), b0 = *(LAS bf16x8*)ph;
    accD = MFMA32(a0, b0, accD);
    if (t < 21) {
      const bf16x8 a1 = tr_frag(pw + 4096, pw + 5120), b1 = *(LAS bf16x8*)(ph + 2048);
      accD = MFMA32(a1, b1, accD);
    }
    slot = slot == (NSLOT2 - 1) * SLOT2 ? 0 : slot + SLOT2;
  }
  STAMP(ws, 10);
#pragma unroll 1
  for (int t = 22; t < NST2; t++) {
    SCHED0;
    if (t <= 30) asm volatile("s_waitcnt vmcnt(14) lgkmcnt(0)\n\ts_barrier" ::: "memory");
    else asm volatile("s_waitcnt vmcnt(0) lgkmcnt(0)\n\ts_barrier" ::: "memory");
    if (t + 8 < NST2) issue(t + 8, slot == 0 ? (NSLOT2 - 1) * SLOT2 : slot - SLOT2);
    LAS char* pq = sm + slot + ha + kh * 2048;
    LAS char* pk = sm + slot + 12288 + kh * 2048 + lane * 16;
    LAS char* pw = sm + slot + wa + kh * 2048;
    f32x16 n = MFMA32(*(LAS bf16x8*)pk, *(LAS bf16x8*)pq, zero);
    n = MFMA32(*(LAS bf16x8*)(pk + 1024), *(LAS bf16x8*)(pq + 1024), n);
    const float rd = frcp(n[8]);
    const u32x4 o = {pk2(n[0] * rd, n[1] * rd), pk2(n[2] * rd, n[3] * rd), pk2(n[4] * rd, n[5] * rd), pk2(n[6] * rd, n[7] * rd)};
    accS = MFMA32(tr_frag(pw, pw + 1024), __builtin_bit_cast(bf16x8, o), accS);
    slot = slot == (NSLOT2 - 1) * SLOT2 ? 0 : slot + SLOT2;
  }
  STAMP(ws, 11);
  __syncthreads();
  {
    LAS f32x4* pd = (LAS f32x4*)(sm + w * 4096 + lane * 16);
    const int o = kh ? 0 : 8;
    f32x16 a = accD, s = accS;
    if (kh) {
      pd[0] = (f32x4){a[0], a[1], a[2], a[3]}; pd[64] = (f32x4){a[4], a[5], a[6], a[7]};
      pd[128] = (f32x4){s[0], s[1], s[2], s[3]}; pd[192] = (f32x4){s[4], s[5], s[6], s[7]};
    } else {
      pd[0] = (f32x4){a[8], a[9], a[10], a[11]}; pd[64] = (f32x4){a[12], a[13], a[14], a[15]};
      pd[128] = (f32x4){s[8], s[9], s[10], s[11]}; pd[192] = (f32x4){s[12], s[13], s[14], s[15]};
    }
    (void)o;
  }
  __syncthreads();
  {
    LAS f32x4* ps = (LAS f32x4*)(sm + (w ^ 4) * 4096 + lane * 16);
    const f32x4 d0 = ps[0], d1 = ps[64], s0 = ps[128], s1 = ps[192];
    float d[8], s[8], xv[8], o[8];
#pragma unroll
    for (int j = 0; j < 4; j++) {
      d[j] = d0[j] + (kh ? accD[8 + j] : accD[j]); d[4 + j] = d1[j] + (kh ? accD[12 + j] : accD[4 + j]);
      s[j] = s0[j] + (kh ? accS[8 + j] : accS[j]); s[4 + j] = s1[j] + (kh ? accS[12 + j] : accS[4 + j]);
    }
    xv[0] = lo_f(xr0[0]); xv[1] = hi_f(xr0[0]); xv[2] = lo_f(xr0[1]); xv[3] = hi_f(xr0[1]);
    xv[4] = lo_f(xr1[0]); xv[5] = hi_f(xr1[0]); xv[6] = lo_f(xr1[1]); xv[7] = hi_f(xr1[1]);
#pragma unroll
    for (int j = 0; j < 8; j++) {
      const float g = d[j] * frcp(1.f + fexp2(-1.4426950408889634f * d[j]));
      o[j] = fmaf(s[j], g, xv[j]);
    }
    *(u32x2*)(out + orow + 4 * h) = (u32x2){pk2(o[0], o[1]), pk2(o[2], o[3])};
    *(u32x2*)(out + orow + 8 + 4 * h) = (u32x2){pk2(o[4], o[5]), pk2(o[6], o[7])};
  }
  STAMP(ws, 12);
}

extern "C" void kernel_launch(void* const* d_in, const int* in_sizes, int n_in, void* d_out, int out_size, void* d_ws,
                              size_t ws_size, hipStream_t stream) {
  const u16* x = (const u16*)d_in[0];
  const u16* wqkv = (const u16*)d_in[1];
  const u16* wphi = (const u16*)d_in[2];
  const u16* wo = (const u16*)d_in[3];
  const u16* wg = (const u16*)d_in[5];
  const u16* wu = (const u16*)d_in[6];
  const u16* wd = (const u16*)d_in[7];
  char* ws = (char*)d_ws;
  k1<<<256, 512, 0, stream>>>(x, wqkv, wphi, wg, wu, ws);
  kred<<<128, 64, 0, stream>>>(ws);
  k2<<<256, 512, 0, stream>>>(x, wo, wd, ws, (u16*)d_out);
}
```

```cpp
#include <hip/hip_runtime.h>
#include <hip/hip_bf16.h>

typedef unsigned short u16;
typedef unsigned int u32;
typedef unsigned long long u64;
typedef short s16x4 __attribute__((ext_vector_type(4)));
typedef __bf16 bf16x8 __attribute__((ext_vector_type(8)));
typedef __bf16 bf16x2 __attribute__((ext_vector_type(2)));
typedef float f32x2 __attribute__((ext_vector_type(2)));
typedef float f32x4 __attribute__((ext_vector_type(4)));
typedef float f32x16 __attribute__((ext_vector_type(16)));
typedef u32 u32x2 __attribute__((ext_vector_type(2)));
typedef u32 u32x4 __attribute__((ext_vector_type(4)));
#define LAS __attribute__((address_space(3)))
#define DI __device__ __forceinline__
#define MFMA32(a, b, c) __builtin_amdgcn_mfma_f32_32x32x16_bf16((a), (b), (c), 0, 0, 0)
#define BF8(x) __builtin_bit_cast(bf16x8, (x))

#define MAP1 1
#define ST_CALC 4
#define ST_GATHER 12

constexpr int DM = 512, FF = 1376, NH = 32, NPAIR = 86;
constexpr size_t WS_H = 0;
constexpr size_t WS_PQ = WS_H + (size_t)64 * 88 * 1024;
constexpr size_t WS_KVP = WS_PQ + (size_t)64 * 32 * 2048;
constexpr size_t WS_KVF = WS_KVP + (size_t)16 * 32 * 4096;
constexpr size_t WS_RS = WS_KVF + 65536;
constexpr size_t WS_F1 = WS_RS + 32768;
constexpr size_t WS_F2 = WS_F1 + 16384;
constexpr size_t WS_EP = WS_F2 + 2048;
constexpr u32 SPIN_CAP = 1u << 15;

#define STAMP(s) do {} while (0)

template <int N> struct IC { static constexpr int value = N; };
template <int B, int E, class F> DI void sfor(F&& f) { if constexpr (B < E) { f(IC<B>{}); sfor<B + 1, E>(f); } }

DI void dma16(const void* sbase, u32 voff, u32 lds) {
  asm volatile("s_mov_b32 m0, %2\n\ts_nop 0\n\tglobal_load_lds_dwordx4 %0, %1" ::"v"(voff), "s"(sbase), "s"(__builtin_amdgcn_readfirstlane(lds)) : "memory");
}
DI void dma16d(const void* sbase, u32 voff, u32 lds) {
  asm volatile("s_mov_b32 m0, %2\n\ts_nop 0\n\tglobal_load_lds_dwordx4 %0, %1 sc1" ::"v"(voff), "s"(sbase), "s"(__builtin_amdgcn_readfirstlane(lds)) : "memory");
}
template <int IMM> DI void set_m0(u32 base) { asm volatile("s_add_u32 m0, %0, %1" ::"s"(base), "n"(IMM) : "memory", "scc"); }
template <int J> DI void dma_p(u32 voff, const void* sbase) {
  asm volatile("global_load_lds_dwordx4 %0, %1 offset:%2" ::"v"(voff), "s"(sbase), "n"(J * 1024) : "memory");
}
template <int J> DI void dma_pm(u32 voff, const void* sbase, u64 em) {
  asm volatile("s_mov_b64 exec, %2\n\tglobal_load_lds_dwordx4 %0, %1 offset:%3\n\ts_mov_b64 exec, -1" ::"v"(voff), "s"(sbase), "s"(em), "n"(J * 1024) : "memory");
}
template <int IMM, int STEP> DI void dma_ring(u32 base, u32& voff, const void* sbase) {
  asm volatile("s_add_u32 m0, %1, %3\n\tv_add_u32 %0, %4, %0\n\tglobal_load_lds_dwordx4 %0, %2 sc1" : "+v"(voff) : "s"(base), "s"(sbase), "n"(IMM), "n"(STEP) : "memory", "scc");
}
DI void dma_ring2(u32 voff, const void* sbase) { asm volatile("global_load_lds_dwordx4 %0, %1 offset:1024 sc1" ::"v"(voff), "s"(sbase) : "memory"); }
DI float max3(float a, float b, float c) { float d; asm("v_max3_f32 %0, %1, %2, %3" : "=v"(d) : "v"(a), "v"(b), "v"(c)); return d; }
DI float nmin3(float a, float b, float c) { float d; asm("v_max3_f32 %0, -%1, -%2, -%3" : "=v"(d) : "v"(a), "v"(b), "v"(c)); return d; }
DI float nmin3m(float m, float b, float c) { float d; asm("v_max3_f32 %0, %1, -%2, -%3" : "=v"(d) : "v"(m), "v"(b), "v"(c)); return d; }
template <int OFF> DI void st16o(void* sbase, u32 voff, const u32x4& v) {
  asm volatile("global_store_dwordx4 %0, %1, %2 offset:%3 sc1\n\ts_nop 1" ::"v"(voff), "v"(v), "s"(sbase), "n"(OFF) : "memory");
}
DI void touch(const void* p, u32 lds) {
  asm volatile("s_mov_b32 m0, %1\n\ts_nop 0\n\tglobal_load_lds_dword %0, off" ::"v"(p), "s"(__builtin_amdgcn_readfirstlane(lds)) : "memory");
}
DI void touchd(const void* sbase, u32 voff, u32 lds) {
  asm volatile("s_mov_b32 m0, %2\n\ts_nop 0\n\tglobal_load_lds_dword %0, %1 sc1" ::"v"(voff), "s"(sbase), "s"(__builtin_amdgcn_readfirstlane(lds)) : "memory");
}
template <int N> DI void lgw() { asm volatile("s_waitcnt lgkmcnt(%0)" ::"n"(N) : "memory"); }
DI f32x2 pkfma(f32x2 a, f32x2 b, f32x2 c) { return __builtin_elementwise_fma(a, b, c); }
DI f32x2 pr(const f32x16& v, int i) { return (f32x2){v[i], v[i + 1]}; }
DI f32x2 sp2(float a) { return (f32x2){a, a}; }
template <int N> DI void vmwait() { asm volatile("s_waitcnt vmcnt(%0)" ::"n"(N) : "memory"); }
DI void lgwait() { asm volatile("s_waitcnt lgkmcnt(0)" ::: "memory"); }
DI void bar() { asm volatile("s_barrier" ::: "memory"); }
DI void st16d(void* sbase, u32 voff, const u32x4& v) { asm volatile("global_store_dwordx4 %0, %1, %2 sc1\n\ts_nop 1" ::"v"(voff), "v"(v), "s"(sbase) : "memory"); }
DI void st8d(void* sbase, u32 voff, const u32x2& v) { asm volatile("global_store_dwordx2 %0, %1, %2 sc1\n\ts_nop 1" ::"v"(voff), "v"(v), "s"(sbase) : "memory"); }
DI void st4d(void* sbase, u32 voff, u32 v) { asm volatile("global_store_dword %0, %1, %2 sc1\n\ts_nop 1" ::"v"(voff), "v"(v), "s"(sbase) : "memory"); }
DI void ld16(u32x4& d, const void* sbase, u32 voff) { asm volatile("global_load_dwordx4 %0, %1, %2" : "+v"(d) : "v"(voff), "s"(sbase) : "memory"); }
DI void ld16d(u32x4& d, const void* sbase, u32 voff) { asm volatile("global_load_dwordx4 %0, %1, %2 sc1" : "+v"(d) : "v"(voff), "s"(sbase) : "memory"); }
DI void ld8d(u32x2& d, const void* sbase, u32 voff) { asm volatile("global_load_dwordx2 %0, %1, %2 sc1" : "=v"(d) : "v"(voff), "s"(sbase) : "memory"); }
template <int OFF> DI void trd(u32x2& d, u32 a) { asm volatile("ds_read_b64_tr_b16 %0, %1 offset:%2" : "=v"(d) : "v"(a), "n"(OFF) : "memory"); }
template <int OFF> DI void rd128(u32x4& d, u32 a) { asm volatile("ds_read_b128 %0, %1 offset:%2" : "=v"(d) : "v"(a), "n"(OFF) : "memory"); }
DI u32x4 cat(const u32x2& a, const u32x2& b) { return (u32x4){a[0], a[1], b[0], b[1]}; }
DI void amfma(f32x16& c, const u32x4& a, const u32x4& b) { asm volatile("v_mfma_f32_32x32x16_bf16 %0, %1, %2, %0" : "+v"(c) : "v"(a), "v"(b)); }
DI void amfma16(f32x4& c, const u32x4& a, const u32x4& b) { asm volatile("v_mfma_f32_16x16x32_bf16 %0, %1, %2, %0" : "+v"(c) : "v"(a), "v"(b)); }
DI void amfma0(f32x16& c, const u32x4& a, const u32x4& b) { asm volatile("v_mfma_f32_32x32x16_bf16 %0, %1, %2, 0" : "=&v"(c) : "v"(a), "v"(b)); }
DI void amfma_n(f32x16& c, const u32x4& a, const u32x4& b) { asm volatile("s_nop 1\n\tv_mfma_f32_32x32x16_bf16 %0, %1, %2, %0" : "+v"(c) : "v"(a), "v"(b)); }

DI bf16x8 tr_frag(LAS char* p0, LAS char* p1) {
  s16x4 a = __builtin_amdgcn_ds_read_tr16_b64_v4i16((LAS s16x4*)p0);
  s16x4 b = __builtin_amdgcn_ds_read_tr16_b64_v4i16((LAS s16x4*)p1);
  return BF8(__builtin_shufflevector(a, b, 0, 1, 2, 3, 4, 5, 6, 7));
}
DI u32 pk2(float a, float b) { f32x2 v = {a, b}; return __builtin_bit_cast(u32, __builtin_convertvector(v, bf16x2)); }
DI u32 pkv(f32x2 v) { return __builtin_bit_cast(u32, __builtin_convertvector(v, bf16x2)); }
DI float bfr(float a) { return __uint_as_float(pk2(a, a) & 0xffff0000u); }
DI float lo_f(u32 w) { return __uint_as_float(w << 16); }
DI float hi_f(u32 w) { return __uint_as_float(w & 0xffff0000u); }
DI float fexp2(float x) { return __builtin_amdgcn_exp2f(x); }
DI float frcp(float x) { return __builtin_amdgcn_rcpf(x); }
DI void swap32(float& a, float& b) {
  auto r = __builtin_amdgcn_permlane32_swap(__float_as_uint(a), __float_as_uint(b), false, false);
  a = __uint_as_float(r[0]); b = __uint_as_float(r[1]);
}
template <int C> DI float dpp_add(float a) { return a + __uint_as_float(__builtin_amdgcn_update_dpp(0, __float_as_uint(a), C, 0xf, 0xf, true)); }
DI float wave_sum(float a) {
  a = dpp_add<0xB1>(a); a = dpp_add<0x4E>(a); a = dpp_add<0x141>(a); a = dpp_add<0x140>(a);
  auto r = __builtin_amdgcn_permlane16_swap(__float_as_uint(a), __float_as_uint(a), false, false);
  a = __uint_as_float(r[0]) + __uint_as_float(r[1]);
  float b = a; swap32(a, b);
  return a + b;
}
DI void poll8(const void* sbase, u32 voff, u32x2 tag) {
#pragma unroll 1
  for (u32 spin = 0; spin < SPIN_CAP; spin++) {
    u32x2 v;
    asm volatile("global_load_dwordx2 %0, %1, %2 sc1\n\ts_waitcnt vmcnt(0)" : "=&v"(v) : "v"(voff), "s"(sbase) : "memory");
    if (__all(v[0] == tag[0] && v[1] == tag[1])) break;
    __builtin_amdgcn_s_sleep(2);
  }
}

DI void ld_f2(u32x4& a, u32x4& b, const void* sbase, u32 voff) {
  asm volatile("global_load_dwordx4 %0, %2, %3 sc1\n\tglobal_load_dwordx4 %1, %2, %3 offset:1024 sc1" : "=&v"(a), "=&v"(b) : "v"(voff), "s"(sbase) : "memory");
}
DI void poll_f2(u32x4& a, u32x4& b, const void* sbase, u32 voff, u32x2 tag) {
  vmwait<0>();
  asm volatile("" : "+v"(a), "+v"(b));
#pragma unroll 1
  for (u32 spin = 0; spin < SPIN_CAP; spin++) {
    const u32 d = ((a[0] ^ tag[0]) | (a[1] ^ tag[1]) | (a[2] ^ tag[0])) | ((a[3] ^ tag[1]) | (b[0] ^ tag[0]) | (b[1] ^ tag[1])) | ((b[2] ^ tag[0]) | (b[3] ^ tag[1]));
    if (__all(d == 0)) break;
    __builtin_amdgcn_s_sleep(2);
    ld_f2(a, b, sbase, voff);
    vmwait<0>();
    asm volatile("" : "+v"(a), "+v"(b));
  }
}

constexpr int SEG1 = 9344, WSLOT = 18688, XRING = 6 * WSLOT, XSLOT = 16384, STAT = XRING + 3 * XSLOT;
constexpr int WPHI_L = XRING + 2 * XSLOT;
constexpr int VIMG_L = XRING, ONES_L = STAT + 1152;
DI constexpr int phik_l(int t) { return t < 6 ? WSLOT + t * 8192 : WPHI_L + (t - 6) * 8192; }
DI constexpr int piece_l(int n) {
  return n < 72 ? ((n / 18 + 4) % 6) * WSLOT + ((n % 18) / 9) * SEG1 + (n % 9) * 1024
       : n < 88 ? XRING + (n - 72) * 1024 : n < 96 ? WPHI_L + 8192 + (n - 88) * 1024
       : n < 132 ? 2 * WSLOT + (n - 96) * 1024 : XRING + XSLOT + (n - 132) * 1024;
}
constexpr int SLOT2 = 12288, WO_L = 8 * SLOT2, REST_L = 6 * SLOT2, LDSB = 163840;
constexpr int R1F = 38;

struct Ctx {
  const u16* seg0; const u16* seg1; const u16* xt; const u16* wphi; const char* wdc; const char *wqkv, *wg, *wu, *wd, *wo;
  int wpo;
  int ld, np, type, ti, jc, p0;
  u32x2 tag;
};

template <bool NORM>
DI void dual_softmax(const f32x16& p, float sc, u32x4 (&fp)[2], u32x4 (&fm)[2]) {
  float mx[2], nn[2], sp[2], sm[2];
  f32x2 ep[2][4], em[2][4];
#pragma unroll
  for (int e = 0; e < 2; e++) {
    const int o = 8 * e;
    mx[e] = max3(max3(max3(p[o], p[o + 1], p[o + 2]), p[o + 3], p[o + 4]), max3(p[o + 5], p[o + 6], p[o + 7]), p[o]);
    nn[e] = nmin3m(nmin3m(nmin3m(nmin3(p[o], p[o + 1], p[o + 2]), p[o + 3], p[o + 4]), p[o + 5], p[o + 6]), p[o + 7], p[o]);
  }
#pragma unroll
  for (int e = 0; e < 2; e++) swap32(mx[e], nn[e]);
#pragma unroll
  for (int e = 0; e < 2; e++) { mx[e] = max3(mx[e], nn[e], nn[e]) * -sc; nn[e] = mx[e]; }
#pragma unroll
  for (int e = 0; e < 2; e++) swap32(mx[e], nn[e]);
#pragma unroll
  for (int e = 0; e < 2; e++)
#pragma unroll
    for (int j = 0; j < 4; j++) { ep[e][j] = pkfma(pr(p, 8 * e + 2 * j), sp2(sc), sp2(mx[e])); em[e][j] = pkfma(pr(p, 8 * e + 2 * j), sp2(-sc), sp2(nn[e])); }
#pragma unroll
  for (int e = 0; e < 2; e++)
#pragma unroll
    for (int j = 0; j < 4; j++) {
      ep[e][j] = (f32x2){fexp2(ep[e][j][0]), fexp2(ep[e][j][1])};
      em[e][j] = (f32x2){fexp2(em[e][j][0]), fexp2(em[e][j][1])};
    }
#pragma unroll
  for (int e = 0; e < 2; e++) {
    const f32x2 tp = (ep[e][0] + ep[e][1]) + (ep[e][2] + ep[e][3]), tm = (em[e][0] + em[e][1]) + (em[e][2] + em[e][3]);
    sp[e] = tp[0] + tp[1]; sm[e] = tm[0] + tm[1];
  }
#pragma unroll
  for (int e = 0; e < 2; e++) swap32(sp[e], sm[e]);
#pragma unroll
  for (int e = 0; e < 2; e++) { sp[e] += sm[e]; if (NORM) sp[e] = frcp(sp[e]); sm[e] = sp[e]; }
#pragma unroll
  for (int e = 0; e < 2; e++) swap32(sp[e], sm[e]);
#pragma unroll
  for (int e = 0; e < 2; e++) {
    if (!NORM) sm[e] = sp[e] * frcp(sm[e]);
#pragma unroll
    for (int j = 0; j < 4; j++) {
      if (NORM) ep[e][j] *= sp2(sp[e]);
      em[e][j] *= sp2(sm[e]);
      fp[e][j] = pkv(ep[e][j]); fm[e][j] = pkv(em[e][j]);
    }
  }
}

template <int NT, bool XR>
DI void mainloop(u32 lb, Ctx& c, char* ws, int w, int lane, f32x16 (&acc)[NT], f32x4& cs0, f32x4& cs1, u32x4& gran) {
  const int tg = w & 3, pg = w >> 2;
  const int r = lane & 31, h = lane >> 5, g16 = (lane >> 4) & 1, q4 = (lane & 15) >> 2, pp = lane & 3;
  const int T0 = pg ? (c.np == 9 ? 5 : 4) : 0;
  const u32 vlin = lane * 16;
  const u32 vw = (lane >> 3) * 1024 + 16 * ((lane & 7) ^ (((lane >> 4) & 1) << 2));
  u32 voff[4];
  const char* src;
  u32 lds, sstep = 0;
  if constexpr (!XR) {
    const int seg = w >= 3, m0 = 3 * (w - 3 * seg), emax = 2 * c.np - 1;
#pragma unroll
    for (int m = 0; m < 3; m++) {
      const int ch = 64 * (m0 + m) + lane, row = (ch * 3641) >> 16;
      int e = ch - row * 18; e = e > emax ? emax : e;
      voff[m] = (u32)(row * c.ld + 8 * e) * 2u - m * 1024;
    }
    voff[3] = 0;
    src = (const char*)(seg ? c.seg1 : c.seg0);
    lds = lb + seg * SEG1 + m0 * 1024;
    sstep = 64 * c.ld;
  } else {
    const int rr = lane >> 3, pos = lane & 7, f = lane >> 4;
    voff[0] = rr * 1024 + 16 * (pos ^ f);
    voff[1] = rr * 1024 + 16 * (pos ^ f ^ 4) + 7168;
    voff[2] = voff[0] + 14336; voff[3] = voff[1] + 14336;
    src = (const char*)c.xt + (w - 6) * 65536;
    lds = lb + XRING + (w - 6) * 8192;
  }
  lds = __builtin_amdgcn_readfirstlane(lds);
  const u64 em = c.type == 2 ? 0ull : ~0ull;
  const char* cur = src;
  auto setup = [&](auto u_) {
    constexpr int u = decltype(u_)::value;
    if constexpr (!XR) {
      constexpr int U = u + 4;
      if constexpr (U == 16) {
        src = c.wdc + 3 * w * 8192; sstep = 18 * 8192;
        voff[0] = vw; voff[1] = vw + 7168; voff[2] = vw + 14336;
      }
      if constexpr (U <= 19) { set_m0<(U % 6) * WSLOT>(lds); cur = src; src += sstep; }
    } else {
      constexpr int sl = (u + 3) >> 1, hf = (u + 3) & 1;
      if constexpr (u == 13) {
        const bool ph = w == 6;
        src = ph ? (const char*)c.wphi : c.wdc + 88 * 8192; sstep = ph ? 4096 : 32768;
#pragma unroll
        for (int j = 0; j < 4; j++) voff[j] = ph ? vlin : vw + j * 7168;
      }
      if constexpr (u == 15) {
        src = c.wdc + (72 + (w - 6) * 8) * 8192; sstep = 32768;
#pragma unroll
        for (int j = 0; j < 4; j++) voff[j] = vw + j * 7168;
      }
      set_m0<(sl % 3) * XSLOT + hf * 4096>(lds);
      if constexpr (u < 13) cur = src + (sl * 128 + hf * 32768);
      else cur = hf ? src + sstep : src;
    }
  };
  auto piece = [&](auto u_, auto j_) {
    constexpr int u = decltype(u_)::value, j = decltype(j_)::value;
    if constexpr (XR ? u <= 16 : (u <= 15 && j < 3)) {
      if constexpr (u >= 15) dma_pm<j>(voff[j], cur, em); else dma_p<j>(voff[j], cur);
    }
  };
  auto unit = [&](auto u_) { setup(u_); asm volatile("s_nop 0"); sfor<0, 4>([&](auto j_) { piece(u_, j_); }); };
  u64 ep;
  asm volatile("s_load_dwordx2 %0, %1, 0x0" : "=s"(ep) : "s"(ws + WS_EP) : "memory");
  u32x4 srow;
  unit(IC<(XR ? -3 : -4)>{});
  asm volatile("global_load_dwordx4 %0, %1, %2" : "=v"(srow) : "v"(vlin), "s"(c.xt + (8 * c.jc + w) * DM) : "memory");
  sfor<(XR ? -2 : -3), 0>(unit);
  const char* wp = nullptr;
  u32 wstep = 0;
  if constexpr (NT == 4 && !XR) {
    const int b = blockIdx.x;
    if (w == 5) {
      const int g = b * 59 + min(lane, 58);
      wp = g < 11008 ? c.wd + g * 128 : c.wo + (g - 11008) * 128;
    } else {
      const int g = min(b * 34 + min(lane, 33), 8575);
      wstep = g < 3072 ? 393216 : 352256;
      wp = (g < 3072 ? c.wqkv + g * 128 : g < 5824 ? c.wg + (g - 3072) * 128 : c.wu + (g - 5824) * 128) + wstep;
      if (w == 4) touch(wp, lb + STAT + 1280);
    }
  }
  u32 wl[NT], wh[NT], xa[4];
#pragma unroll
  for (int s = 0; s < NT; s++) {
    int t = s + tg; t = t >= NT ? t - NT : t;
    wl[s] = lb + g16 * SEG1 + (8 * h + q4) * 288 + 8 * pp + 32 * (T0 + t);
    wh[s] = wl[s] + 3 * WSLOT;
#pragma unroll
    for (int j = 0; j < 16; j++) acc[s][j] = 0.f;
    asm volatile("" : "+v"(acc[s]), "+v"(wl[s]), "+v"(wh[s]));
  }
#pragma unroll
  for (int k = 0; k < 4; k++) { xa[k] = (lb + XRING + (32 * tg + r) * 128 + 16 * (h ^ ((r >> 1) & 7))) ^ (32 * k); asm volatile("" : "+v"(xa[k])); }
  cs0 = (f32x4){0.f, 0.f, 0.f, 0.f}; cs1 = cs0;
  const u32 one = ((lane & 31) == 0 || (lane & 31) == 17) ? 0x3f803f80u : 0u;
  u32x4 sel = {one, one, one, one};
  u32 gaddr = r * 16, vw_ = vw;
  asm volatile("s_waitcnt lgkmcnt(0)" : "+s"(ep), "+v"(cs0), "+v"(cs1), "+v"(sel), "+v"(gaddr), "+v"(vw_), "+v"(wp), "+v"(wstep) :: "memory");
  ep += 1;
  c.tag = (u32x2){(u32)ep, (u32)(ep >> 32)};
  u32x2 A[2][NT][2];
  u32x4 B[2];
  auto rdw = [&](auto k_, auto s_) {
    constexpr int k = decltype(k_)::value, s = decltype(s_)::value, sl = (k >> 1) % 6, O = (sl % 3) * WSLOT + (k & 1) * 4608;
    trd<O>(A[k & 1][s][0], sl < 3 ? wl[s] : wh[s]); trd<O + 1152>(A[k & 1][s][1], sl < 3 ? wl[s] : wh[s]);
  };
  auto rdx = [&](auto k_) { constexpr int k = decltype(k_)::value; rd128<((k >> 2) % 3) * XSLOT>(B[k & 1], xa[k & 3]); };
  if constexpr (XR) vmwait<4>(); else vmwait<9>();
  bar();
  STAMP(1);
  rdx(IC<0>{});
  sfor<0, NT>([&](auto s_) { rdw(IC<0>{}, s_); });
  setup(IC<0>{}); asm volatile("s_nop 0"); piece(IC<0>{}, IC<0>{}); piece(IC<0>{}, IC<1>{});
  sfor<0, 32>([&](auto k_) {
    constexpr int k = decltype(k_)::value, C = k & 1;
    constexpr int u = (k + 1) >> 1;
    constexpr bool N = k < 31;
    using K1 = IC<k + 1>;
    lgwait();
    amfma(acc[0], cat(A[C][0][0], A[C][0][1]), B[C]);
    if constexpr (C && u < 16) {
      if constexpr (XR) { if constexpr (!(u & 1)) vmwait<4>(); } else vmwait<9>();
      bar();
    }
    if constexpr (k == 31 && XR) { vmwait<4>(); if (c.type == 2) vmwait<0>(); }
    if constexpr (k == 31) bar();
    if constexpr (C) {
      if constexpr (NT == 4 && !XR && (u == 1 || u == 3)) { if (w == 4) { wp += wstep; touch(wp, lb + STAT + 1280); } }
      if constexpr (NT == 4 && !XR && u == 7) { if (w == 5) touch(wp, lb + STAT + 1280); }
      if constexpr (u == ST_GATHER) asm volatile("global_load_dwordx4 %0, %1, %2 sc1" : "=v"(gran) : "v"(gaddr), "s"(ws + WS_RS + (128 * c.ti + 32 * tg) * 16) : "memory");
      setup(IC<u>{});
    }
    if constexpr (N) { rdx(K1{}); rdw(K1{}, IC<0>{}); }
    piece(IC<u>{}, IC<(C ? 0 : 2)>{});
    amfma(acc[1], cat(A[C][1][0], A[C][1][1]), B[C]);
    if constexpr (N) { rdw(K1{}, IC<1>{}); rdw(K1{}, IC<2>{}); }
    amfma16(cs0, sel, cat(A[C][0][0], A[C][0][1]));
    amfma(acc[2], cat(A[C][2][0], A[C][2][1]), B[C]);
    if constexpr (N) { rdw(K1{}, IC<3>{}); if constexpr (NT == 5) rdw(K1{}, IC<NT - 1>{}); }
    piece(IC<u>{}, IC<(C ? 1 : 3)>{});
    amfma(acc[3], cat(A[C][3][0], A[C][3][1]), B[C]);
    if constexpr (NT == 5) {
      amfma16(cs1, sel, cat(A[C][1][0], A[C][1][1]));
      amfma(acc[NT - 1], cat(A[C][NT - 1][0], A[C][NT - 1][1]), B[C]);
    }
    if constexpr (k == 2 * ST_CALC - 1) {
      asm volatile("" : "+v"(srow));
      const float piv = lo_f(__builtin_amdgcn_readfirstlane(srow[0]));
      f32x2 v[4];
#pragma unroll
      for (int j = 0; j < 4; j++) v[j] = (f32x2){lo_f(srow[j]), hi_f(srow[j])} - sp2(piv);
      const f32x2 s2 = (v[0] + v[1]) + (v[2] + v[3]), q2 = pkfma(v[0], v[0], v[1] * v[1]) + pkfma(v[2], v[2], v[3] * v[3]);
      const float ms = wave_sum(s2[0] + s2[1]) * (1.f / 512.f), mq = wave_sum(q2[0] + q2[1]) * (1.f / 512.f);
      const float mean = piv + ms, var = fmaxf(mq - ms * ms, 0.f);
      const float sd = bfr(__builtin_amdgcn_sqrtf(bfr(bfr(var) + 1e-5f)));
      st16d(ws + WS_RS + (128 * c.ti + 8 * c.jc + w) * 16, 0u, (u32x4){__float_as_uint(sd), __float_as_uint(bfr(mean)), c.tag[0], c.tag[1]});
    }
  });
  piece(IC<16>{}, IC<2>{}); piece(IC<16>{}, IC<3>{});
  if constexpr (XR) vmwait<4 * (17 - ST_GATHER)>();
  else { if (c.type == 2) vmwait<3 * (15 - ST_GATHER)>(); else vmwait<3 * (16 - ST_GATHER)>(); }
  asm volatile("s_nop 15" : "+v"(cs0), "+v"(cs1), "+v"(gran) :: "memory");
#pragma unroll
  for (int s = 0; s < NT; s++) asm volatile("" : "+v"(acc[s]));
}

template <int NT>
DI void stage1(LAS char* sm, u32 lb, Ctx& c, char* ws, int w, int lane) {
  const int tg = w & 3, pg = w >> 2;
  const int r = lane & 31, h = lane >> 5, g16 = (lane >> 4) & 1, q4 = (lane & 15) >> 2, pp = lane & 3;
  const int T0 = pg ? (c.np == 9 ? 5 : 4) : 0;
  const u32 vlin = lane * 16;
  const u32 vw = (lane >> 3) * 1024 + 16 * ((lane & 7) ^ (((lane >> 4) & 1) << 2));
  f32x16 acc[NT];
  f32x4 cs0, cs1;
  u32x4 gran;
  if (NT == 4 && w >= 6) mainloop<NT, true>(lb, c, ws, w, lane, acc, cs0, cs1, gran);
  else mainloop<NT, false>(lb, c, ws, w, lane, acc, cs0, cs1, gran);
  STAMP(2);
  if (lane < 16) {
    LAS float* cs = (LAS float*)(sm + STAT);
    cs[(T0 + tg) * 32 + lane] = cs0[0]; cs[(T0 + tg) * 32 + 16 + lane] = cs0[1];
    if (NT == 5 && tg == 3) { cs[128 + lane] = cs1[0]; cs[144 + lane] = cs1[1]; }
  }
  u32x4 wab[4];
  if (NT == 4 && c.type) {
    LAS char* wp = sm + WPHI_L + c.wpo + (4 * h + q4) * 32 + 8 * pp;
#pragma unroll
    for (int s = 0; s < 4; s++) {
      const int t = c.type == 1 ? T0 + ((s + tg) & 3) + 8 * g16 : T0 + (((s & 2) + g16 + tg) & 3);
      wab[s] = __builtin_bit_cast(u32x4, tr_frag(wp + t * 512, wp + t * 512 + 256));
    }
  }
#pragma unroll 1
  for (u32 spin = 0; spin < SPIN_CAP && !__all(gran[2] == c.tag[0] && gran[3] == c.tag[1]); spin++) {
    __builtin_amdgcn_s_sleep(2);
    asm volatile("global_load_dwordx4 %0, %1, %2 sc1\n\ts_waitcnt vmcnt(0)" : "=&v"(gran) : "v"(r * 16), "s"(ws + WS_RS + (128 * c.ti + 32 * tg) * 16) : "memory");
  }
  const float sd = __uint_as_float(gran[0]), nmean = -__uint_as_float(gran[1]), rs = 1.f / sd;
  lgwait();
  bar();
  auto w2 = [&](int j) { const int n = 96 + w + 8 * j; dma16(c.wdc + n * 8192, vw, lb + piece_l(n)); };
#pragma unroll
  for (int s = 0; s < NT; s++) {
    int t = s + tg; t = t >= NT ? t - NT : t;
    LAS f32x4* cp = (LAS f32x4*)(sm + STAT + ((T0 + t) * 32 + 4 * h) * 4);
#pragma unroll
    for (int i = 0; i < 4; i++) {
      const f32x4 cv = cp[2 * i];
      const f32x2 a0 = pkfma(sp2(nmean), (f32x2){cv[0], cv[1]}, pr(acc[s], 4 * i)), a1 = pkfma(sp2(nmean), (f32x2){cv[2], cv[3]}, pr(acc[s], 4 * i + 2));
      acc[s][4 * i] = a0[0]; acc[s][4 * i + 1] = a0[1]; acc[s][4 * i + 2] = a1[0]; acc[s][4 * i + 3] = a1[1];
    }
  }
  const int tg32 = 4 * c.ti + tg;
  if (c.type == 0) {
    const float K0 = -2.f * 0.7978845608028654f * 1.4426950408889634f * rs, K1 = K0 * 0.044715f * rs * rs, v2 = sd * sd;
    char* dst = ws + WS_H + (size_t)(tg32 * NPAIR + c.p0 + T0) * 1024;
#pragma unroll
    for (int s = 0; s < NT; s++) {
      int t = s + tg; t = t >= NT ? t - NT : t;
      if (s < 4) { w2(2 * s); if (s < 3) w2(2 * s + 1); }
      f32x2 e[4], gu[4];
#pragma unroll
      for (int j = 0; j < 4; j++) { const f32x2 g = pr(acc[s], 2 * j); e[j] = g * pkfma(g * g, sp2(K1), sp2(K0)); gu[j] = g * pr(acc[s], 8 + 2 * j); }
#pragma unroll
      for (int j = 0; j < 4; j++) e[j] = pkfma((f32x2){fexp2(e[j][0]), fexp2(e[j][1])}, sp2(v2), sp2(v2));
#pragma unroll
      for (int j = 0; j < 4; j++) gu[j] *= (f32x2){frcp(e[j][0]), frcp(e[j][1])};
      st16d(dst, vlin + t * 1024, (u32x4){pkv(gu[0]), pkv(gu[1]), pkv(gu[2]), pkv(gu[3])});
    }
  } else if constexpr (NT == 4) {
    const float sc = rs * 1.4426950408889634f;
    f32x16 zero;
#pragma unroll
    for (int j = 0; j < 16; j++) zero[j] = 0.f;
    if (c.type == 1) {
      char* dst = ws + WS_PQ + (size_t)(tg32 * NH + 16 * (c.jc - 10) + T0) * 2048;
      f32x16 P[4];
#pragma unroll
      for (int s = 0; s < 4; s++) {
        u32x4 wA, wB, qa, qb;
#pragma unroll
        for (int j = 0; j < 4; j++) {
          wA[j] = g16 ? 0u : wab[s][j]; wB[j] = g16 ? wab[s][j] : 0u;
          qa[j] = pkv(pr(acc[s], 2 * j)); qb[j] = pkv(pr(acc[s], 8 + 2 * j));
        }
        P[s] = MFMA32(BF8(wA), BF8(qa), zero);
        P[s] = MFMA32(BF8(wB), BF8(qb), P[s]);
      }
#pragma unroll
      for (int s = 0; s < 4; s++) {
        const u32 vo = vlin + ((s + tg) & 3) * 2048;
        w2(2 * s); if (s < 3) w2(2 * s + 1);
        u32x4 fp[2], fm[2];
        dual_softmax<false>(P[s], sc, fp, fm);
        st16o<0>(dst, vo, fp[0]); st16o<1024>(dst, vo, fm[0]);
        st16o<0>(dst + 16384, vo, fp[1]); st16o<1024>(dst + 16384, vo, fm[1]);
      }
    } else {
      const int tok = 32 * tg + r;
      const u32 pw = tok * 64, vwl = VIMG_L + tok * 32;
      const int fsw = (r >> 1) & 7, vsw = (r >> 2) & 3;
      f32x16 P[2];
#pragma unroll
      for (int s = 0; s < 4; s += 2) {
        u32x4 wA, wB, ka, kb;
#pragma unroll
        for (int j = 0; j < 4; j++) {
          wA[j] = g16 ? 0u : wab[s][j]; wB[j] = g16 ? wab[s][j] : 0u;
          ka[j] = pkv(pr(acc[s], 2 * j)); kb[j] = pkv(pr(acc[s + 1], 2 * j));
        }
        P[s >> 1] = MFMA32(BF8(wA), BF8(ka), zero);
        P[s >> 1] = MFMA32(BF8(wB), BF8(kb), P[s >> 1]);
      }
#pragma unroll
      for (int s = 0; s < 4; s++) {
        LAS char* vd = sm + vwl + (T0 + ((s + tg) & 3)) * 4096;
        const f32x16& a = acc[s];
        *(LAS u32x2*)(vd + 8 * ((0 + h) ^ vsw)) = (u32x2){pkv(pr(a, 8) * sp2(rs)), pkv(pr(a, 10) * sp2(rs))};
        *(LAS u32x2*)(vd + 8 * ((2 + h) ^ vsw)) = (u32x2){pkv(pr(a, 12) * sp2(rs)), pkv(pr(a, 14) * sp2(rs))};
      }
#pragma unroll
      for (int s = 0; s < 4; s += 2) {
        u32x4 fp[2], fm[2];
        dual_softmax<true>(P[s >> 1], sc, fp, fm);
#pragma unroll
        for (int e = 0; e < 2; e++) {
          LAS char* pd = sm + pw + phik_l(T0 + ((s + e + tg) & 3));
          *(LAS u32x2*)(pd + 8 * ((0 + h) ^ fsw)) = (u32x2){fp[e][0], fp[e][1]};
          *(LAS u32x2*)(pd + 8 * ((2 + h) ^ fsw)) = (u32x2){fp[e][2], fp[e][3]};
          *(LAS u32x2*)(pd + 8 * ((4 + h) ^ fsw)) = (u32x2){fm[e][0], fm[e][1]};
          *(LAS u32x2*)(pd + 8 * ((6 + h) ^ fsw)) = (u32x2){fm[e][2], fm[e][3]};
        }
      }
      STAMP(12);
      lgwait();
      bar();
      f32x16 kv = zero;
      {
        const int t0 = 8 * h + q4;
        const u32 pa0 = lb + phik_l(w) + t0 * 64 + 8 * ((4 * g16 + pp) ^ (t0 >> 1)), pa1 = lb + phik_l(w) + (t0 + 4) * 64 + 8 * ((4 * g16 + pp) ^ ((t0 >> 1) + 2));
        const u32 pb0 = lb + VIMG_L + w * 4096 + t0 * 32 + 8 * (pp ^ (2 * h)), pb1 = lb + VIMG_L + w * 4096 + (t0 + 4) * 32 + 8 * (pp ^ (2 * h + 1));
        u32x2 fa[8][2], fb[8][2];
        sfor<0, 8>([&](auto k_) {
          constexpr int k = decltype(k_)::value;
          trd<k * 1024>(fa[k][0], pa0); trd<k * 1024>(fa[k][1], pa1); trd<k * 512>(fb[k][0], pb0); trd<k * 512>(fb[k][1], pb1);
        });
        lgwait();
        sfor<0, 8>([&](auto k_) {
          constexpr int k = decltype(k_)::value;
          asm volatile("" : "+v"(fa[k][0]), "+v"(fa[k][1]), "+v"(fb[k][0]), "+v"(fb[k][1]));
          u32x4 bq = cat(fb[k][0], fb[k][1]);
#pragma unroll
          for (int j = 0; j < 4; j++) bq[j] = g16 ? 0x3f803f80u : bq[j];
          kv = MFMA32(BF8(cat(fa[k][0], fa[k][1])), BF8(bq), kv);
        });
      }
      if (r <= 16) {
        char* dst = ws + WS_KVP + (size_t)(c.ti * NH + 8 * (c.jc - 12) + w) * 4096;
#pragma unroll
        for (int g = 0; g < 4; g++)
          st16d(dst + g * 1024, vlin, (u32x4){__float_as_uint(kv[4 * g]), __float_as_uint(kv[4 * g + 1]), __float_as_uint(kv[4 * g + 2]), __float_as_uint(kv[4 * g + 3])});
      }
      STAMP(13);
      vmwait<0>();
      lgwait();
      bar();
      if (w == 0) st8d(ws + WS_F1 + (c.ti * 16 + c.jc) * 64, 0u, c.tag);
      STAMP(14);
#pragma unroll 1
      for (int n = 54 + w; n < 152; n += 8) dma16(c.wdc + n * 8192, vw, lb + piece_l(n));
    }
  }
}

constexpr int ops_after(bool lo, int t) {
  if (t < 0) return lo ? 12 : 6;
  int n = 0;
  for (int st = (t == 0 ? 6 : t + 7); st <= t + 7; st++) {
    if (st <= 14) n += lo ? 2 : 1;
    else if (st <= 30) n += lo ? 0 : 2;
  }
  if (lo && t == 9) n += 2;
  if (lo && t == 12) n += 5;
  if (lo && t > 12 && t + 7 <= 30) n += 1;
  return n;
}
constexpr int need_n(bool lo, int t) {
  int last = 0;
  int tail = 0;
  if (t <= 5) { last = -1; tail = (5 - t) * (lo ? 2 : 1); }
  else if (t == 6) { last = 0; tail = lo ? 2 : 1; }
  else if (lo && t >= 15 && t <= 19) { last = 12; tail = 19 - t; }
  else last = t - 7;
  int n = tail;
  for (int k = last + 1; k < t; k++) n += ops_after(lo, k);
  return n;
}

template <bool HI>
DI void stage2(LAS char* sm, u32 lb, const Ctx& c, char* ws, int w, int lane, int ti2, int cj, const u16* x, const u16* wo, u16* out) {
  const int tg = w & 1, cg = (w >> 1) & 1, b = blockIdx.x;
  constexpr int kh = HI;
  const int r = lane & 31, h = lane >> 5, g16 = (lane >> 4) & 1, q4 = (lane & 15) >> 2, pp = lane & 3;
  const u32 vlin = lane * 16;
  const u32 vw = (lane >> 3) * 1024 + 16 * ((lane & 7) ^ (((lane >> 4) & 1) << 2));
  const u32 wa0 = lb + (4 * h + q4) * 128 + ((cg ^ (q4 >> 1)) << 6) + 32 * g16 + 8 * pp, wa = wa0 + kh * 2048;
  const int khead = b >> 3, kg = (b >> 1) & 3, khalf = b & 1;
  u32x2 part[16];
  if (HI && w == 7) {
    poll8(ws + WS_F1 + (12 + (khead >> 3)) * 64, (lane & 15) * 1024, c.tag);
    const char* src = ws + WS_KVP + khead * 4096 + kg * 1024 + khalf * 8;
    const u32 vo = (r > 16 ? (lane & 32) + 16 : lane) * 16;
#pragma unroll
    for (int i = 0; i < 16; i++) ld8d(part[i], src + (size_t)i * NH * 4096, vo);
  }
  u32x2 wf[43][2];
  auto readout = [&](auto k_) {
    sfor<0, R1F>([&](auto i) {
      constexpr int I = decltype(i)::value, n = 4 * I + 2 * decltype(k_)::value, O0 = piece_l(n), O1 = piece_l(n + 1);
      trd<(O0 & 0xffff)>(wf[I][0], wa0 + (O0 & ~0xffff)); trd<(O1 & 0xffff)>(wf[I][1], wa0 + (O1 & ~0xffff));
    });
    lgwait();
  };
  readout(IC<kh>{});
  if (HI && w == 7) {
    vmwait<0>();
    float s0 = 0.f, s1 = 0.f;
#pragma unroll
    for (int i = 0; i < 16; i++) { asm volatile("" : "+v"(part[i])); s0 += __uint_as_float(part[i][0]); s1 += __uint_as_float(part[i][1]); }
    if (r >= 16 && kg < 2) { s0 += 1e-6f; s1 += 1e-6f; }
    st4d(ws + WS_KVF + khead * 2048 + (kg >> 1) * 1024 + (kg & 1) * 8 + khalf * 4, vlin, pk2(s0, s1));
  }
  lgwait();
  bar();
  STAMP(5);
  if (HI || w) {
#pragma unroll 1
    for (int m = 0; m < 12; m++) {
      const int n = w - 1 + 7 * m;
      if (n < 20) dma16(c.wdc + (152 + n) * 8192, vw, lb + REST_L + n * 1024);
      else dma16((const char*)wo + (n - 20) * 8192 + cj * 128, vw, lb + WO_L + (n - 20) * 1024);
    }
    if (HI && w == 7) { vmwait<12>(); st8d(ws + WS_F2 + b * 8, 0u, c.tag); }
    vmwait<9>();
  } else {
    poll8(ws + WS_F1 + (ti2 >> 1) * 1024, (lane & 15) < 12 ? (lane & 15) * 64 : 0, c.tag);
  }
  bar();
  STAMP(6);
  const size_t orow = (size_t)(64 * ti2 + 32 * tg + r) * DM + 64 * cj + 32 * cg + 16 * kh;
  const u32x2 xr0 = *(const u32x2*)(x + orow + 4 * h), xr1 = *(const u32x2*)(x + orow + 8 + 4 * h);
  f32x16 accD, accS;
#pragma unroll
  for (int j = 0; j < 16; j++) { accD[j] = 0.f; accS[j] = 0.f; }
  {
    const int p0 = HI ? w + 4 : 2 * w;
    const char* hS = ws + WS_H + (size_t)((2 * ti2 + (p0 >= 6)) * NPAIR + (p0 >= 6 ? p0 - 6 : p0)) * 1024;
    const char* aS = HI ? ws + WS_PQ + (size_t)(2 * ti2 + ((w >> 1) & 1)) * 65536 + (w & 1) * 2048 : ws + WS_KVF + w * 1024;
    const u32 mh = __builtin_amdgcn_readfirstlane(lb + p0 * 1024), ma = __builtin_amdgcn_readfirstlane(lb + (HI ? (w - 4) * 2048 : 8192 + w * 1024));
    u32 vh = vlin - 6144, va = vlin - 4096;
    auto issue_a = [&](auto st_) {
      constexpr int st = decltype(st_)::value;
      if constexpr (st <= 14) dma_ring<(st & 7) * SLOT2, 6144>(mh, vh, hS);
      else if constexpr (st <= 30 && HI) dma_ring<(st & 7) * SLOT2, 4096>(ma, va, aS);
    };
    auto issue_b = [&](auto st_) {
      constexpr int st = decltype(st_)::value;
      if constexpr (st <= 14) { if constexpr (!HI) dma_ring2(vh, hS); }
      else if constexpr (st <= 30 && HI) dma_ring2(va, aS);
    };
    auto issue_k = [&](auto st_) {
      constexpr int st = decltype(st_)::value;
      if constexpr (st >= 15 && st <= 30 && !HI) dma_ring<(st & 7) * SLOT2, 4096>(ma, va, aS);
    };
    sfor<0, 6>([&](auto t_) { issue_a(t_); issue_b(t_); });
    sfor<R1F, 43>([&](auto i) {
      constexpr int I = decltype(i)::value, O = (I - R1F) * 4096;
      trd<O>(wf[I][0], wa + REST_L); trd<O + 1024>(wf[I][1], wa + REST_L);
    });
    u32 ha[2], qa[2], ka[2], woa = wa + WO_L;
    ha[0] = lb + tg * 6144 + HI * 1024 + vlin; qa[0] = lb + tg * 4096 + HI * 2048 + vlin; ka[0] = lb + 8192 + HI * 2048 + vlin;
    ha[1] = ha[0] + 4 * SLOT2; qa[1] = qa[0] + 4 * SLOT2; ka[1] = ka[0] + 4 * SLOT2;
    u32x4 hf[2][3], f2a, f2b;
    u32x4 pq[2][2], kv[2][2];
    u32x2 wof[2][2];
    f32x16 nn[2];
    asm volatile("" : "+v"(accD), "+v"(accS), "+v"(ha[0]), "+v"(ha[1]), "+v"(qa[0]), "+v"(qa[1]), "+v"(ka[0]), "+v"(ka[1]), "+v"(woa));
    sfor<0, 33>([&](auto t_) {
      constexpr int t = decltype(t_)::value, p = t - 1, pp2 = t - 2;
      lgwait();
      if constexpr (p >= 0 && p <= 14) amfma(accD, cat(wf[3 * p][0], wf[3 * p][1]), hf[p & 1][0]);
      if constexpr (p >= 15 && p <= 30) amfma0(nn[p & 1], kv[p & 1][0], pq[p & 1][0]);
      if constexpr (t == 12 && !HI) {
        if (w == 0) poll_f2(f2a, f2b, ws + WS_F2, vlin, c.tag);
      }
      if constexpr (t <= 30) {
        vmwait<need_n(!HI, t)>();
        bar();
        if constexpr (t == 0) STAMP(7);
        if constexpr (t == 12) STAMP(8);
        if constexpr (t == 15) STAMP(9);
        constexpr int so = (t & 3) * SLOT2, hb = (t >> 2) & 1;
        if constexpr (t <= 14) {
          rd128<so>(hf[t & 1][0], ha[hb]);
          if constexpr (t < 14) { rd128<so + 2048>(hf[t & 1][1], ha[hb]); rd128<so + 4096>(hf[t & 1][2], ha[hb]); }
        } else {
          rd128<so>(pq[t & 1][0], qa[hb]); rd128<so>(kv[t & 1][0], ka[hb]);
          rd128<so + 1024>(pq[t & 1][1], qa[hb]); rd128<so + 1024>(kv[t & 1][1], ka[hb]);
        }
      }
      if constexpr (p >= 15 && p <= 30) { trd<(p - 15) * 4096>(wof[p & 1][0], woa); trd<(p - 15) * 4096 + 1024>(wof[p & 1][1], woa); }
      if constexpr (p >= 0 && p < 14) amfma(accD, cat(wf[3 * p + 1][0], wf[3 * p + 1][1]), hf[p & 1][1]);
      if constexpr (p >= 15 && p <= 30) amfma(nn[p & 1], kv[p & 1][1], pq[p & 1][1]);
      if constexpr (t == 0) { issue_a(IC<6>{}); issue_b(IC<6>{}); }
      issue_a(IC<t + 7>{});
      if constexpr (t == 12) sfor<15, 19>(issue_k);
      if constexpr (t >= 12) issue_k(IC<t + 7>{});
      if constexpr (p >= 0 && p < 14) amfma(accD, cat(wf[3 * p + 2][0], wf[3 * p + 2][1]), hf[p & 1][2]);
      if constexpr (pp2 >= 15 && pp2 <= 30) {
        f32x16& n = nn[pp2 & 1];
        asm volatile("" : "+v"(n));
        const f32x2 rd = sp2(frcp(n[8]));
        const u32x4 o = {pkv(pr(n, 0) * rd), pkv(pr(n, 2) * rd), pkv(pr(n, 4) * rd), pkv(pr(n, 6) * rd)};
        amfma_n(accS, cat(wof[pp2 & 1][0], wof[pp2 & 1][1]), o);
      }
      issue_b(IC<t + 7>{});
      if constexpr (t == 9 && !HI) ld_f2(f2a, f2b, ws + WS_F2, vlin);
      if constexpr (t == 31) asm volatile("s_nop 11");
    });
  }
  asm volatile("s_nop 15" : "+v"(accD), "+v"(accS));
  STAMP(10);
  {
    LAS f32x4* pd = (LAS f32x4*)(sm + w * 4096 + lane * 16);
    if (kh) {
      pd[0] = (f32x4){accD[0], accD[1], accD[2], accD[3]}; pd[64] = (f32x4){accD[4], accD[5], accD[6], accD[7]};
      pd[128] = (f32x4){accS[0], accS[1], accS[2], accS[3]}; pd[192] = (f32x4){accS[4], accS[5], accS[6], accS[7]};
    } else {
      pd[0] = (f32x4){accD[8], accD[9], accD[10], accD[11]}; pd[64] = (f32x4){accD[12], accD[13], accD[14], accD[15]};
      pd[128] = (f32x4){accS[8], accS[9], accS[10], accS[11]}; pd[192] = (f32x4){accS[12], accS[13], accS[14], accS[15]};
    }
  }
  lgwait();
  bar();
  {
    LAS f32x4* ps = (LAS f32x4*)(sm + (w ^ 4) * 4096 + lane * 16);
    const f32x4 d0 = ps[0], d1 = ps[64], s0 = ps[128], s1 = ps[192];
    float d[8], s[8], xv[8], o[8];
#pragma unroll
    for (int j = 0; j < 4; j++) {
      d[j] = d0[j] + (kh ? accD[8 + j] : accD[j]); d[4 + j] = d1[j] + (kh ? accD[12 + j] : accD[4 + j]);
      s[j] = s0[j] + (kh ? accS[8 + j] : accS[j]); s[4 + j] = s1[j] + (kh ? accS[12 + j] : accS[4 + j]);
    }
    xv[0] = lo_f(xr0[0]); xv[1] = hi_f(xr0[0]); xv[2] = lo_f(xr0[1]); xv[3] = hi_f(xr0[1]);
    xv[4] = lo_f(xr1[0]); xv[5] = hi_f(xr1[0]); xv[6] = lo_f(xr1[1]); xv[7] = hi_f(xr1[1]);
#pragma unroll
    for (int j = 0; j < 8; j++) {
      const float g = d[j] * frcp(1.f + fexp2(-1.4426950408889634f * d[j]));
      o[j] = fmaf(s[j], g, xv[j]);
    }
    *(u32x2*)(out + orow + 4 * h) = (u32x2){pk2(o[0], o[1]), pk2(o[2], o[3])};
    *(u32x2*)(out + orow + 8 + 4 * h) = (u32x2){pk2(o[4], o[5]), pk2(o[6], o[7])};
  }
  if constexpr (HI) asm volatile(".Lfused_end1:"); else asm volatile(".Lfused_end0:");
  if (!HI && b == 0 && w == 1) {
#pragma unroll 1
    for (int i = 0; i < 4; i++) poll8(ws + WS_F1 + i * 4096, lane * 64, c.tag);
    st8d(ws + WS_EP, 0u, c.tag);
  }
  STAMP(11);
}

__global__ __launch_bounds__(512, 2) void fused(const u16* __restrict__ x, const u16* __restrict__ wqkv, const u16* __restrict__ wphi,
                                                const u16* __restrict__ wo, const u16* __restrict__ wg, const u16* __restrict__ wu,
                                                const u16* __restrict__ wd, char* __restrict__ ws, u16* __restrict__ out) {
  __shared__ __attribute__((aligned(256))) char smem[LDSB];
  LAS char* sm = (LAS char*)smem;
  const u32 lb = (u32)(size_t)sm;
  const int b = blockIdx.x, xcd = b & 7, loc = b >> 3;
  const int w = __builtin_amdgcn_readfirstlane(threadIdx.x >> 6), lane = threadIdx.x & 63;
  asm volatile("" ::"s"(x), "s"(wqkv), "s"(wphi), "s"(wo), "s"(wg), "s"(wu), "s"(wd), "s"(ws), "s"(out));
  STAMP(0);
  if (w == 7) {
    u64 pc; u32 len, len1;
    asm volatile("s_getpc_b64 %0\n.Lfused_pc:\n\ts_mov_b32 %1, .Lfused_end0-.Lfused_pc\n\ts_mov_b32 %2, .Lfused_end1-.Lfused_pc" : "=s"(pc), "=s"(len), "=s"(len1));
    len = max(len, len1);
    const u32 lim = (len & ~15u) - 16u;
    dma16((const char*)(pc & ~15ull), min((u32)(loc * 1024 + lane * 16), lim), lb + STAT);
    dma16((const char*)(pc & ~15ull), min((u32)(loc * 1024 + lane * 16 + 32768), lim), lb + STAT);
    dma16((const char*)(pc & ~15ull), min((u32)(loc * 1024 + lane * 16 + 65536), lim), lb + STAT);
  }
  Ctx c;
  c.ti = 2 * xcd + (loc >> 4); c.jc = loc & 15;
  const int ti2 = 4 * xcd + (loc >> 3), cj = loc & 7;
  c.xt = x + (size_t)c.ti * 128 * DM;
  c.wphi = wphi; c.p0 = 0; c.wpo = 0;
  c.wdc = (const char*)wd + cj * 128;
  c.wqkv = (const char*)wqkv; c.wg = (const char*)wg; c.wu = (const char*)wu; c.wd = (const char*)wd; c.wo = (const char*)wo;
  if (c.jc < 10) {
    c.type = 0; c.ld = FF; c.np = c.jc < 6 ? 9 : 8;
    c.p0 = c.jc < 6 ? 9 * c.jc : 54 + 8 * (c.jc - 6);
    c.seg0 = wg + 16 * c.p0; c.seg1 = wu + 16 * c.p0;
  } else if (c.jc < 12) {
    c.type = 1; c.ld = 3 * DM; c.np = 8;
    c.seg0 = wqkv + 256 * (c.jc - 10); c.seg1 = c.seg0 + 128;
    c.wphi = wphi + (c.jc - 10) * 16 * 256;
  } else {
    c.type = 2; c.ld = 3 * DM; c.np = 8;
    c.seg0 = wqkv + DM + 128 * (c.jc - 12); c.seg1 = c.seg0 + DM;
    c.wphi = wphi + (c.jc == 15 ? 2 : c.jc - 12) * 8 * 256; c.wpo = c.jc == 15 ? 4096 : 0;
  }
  if (c.np == 9 && w < 4) stage1<5>(sm, lb, c, ws, w, lane);
  else stage1<4>(sm, lb, c, ws, w, lane);
  STAMP(3);
  vmwait<0>();
  bar();
  if (w == 0 && c.type != 2) st8d(ws + WS_F1 + (c.ti * 16 + c.jc) * 64, 0u, c.tag);
  STAMP(4);
  if (c.type == 0) touchd(ws + WS_H + (size_t)((4 * c.ti + (w & 3)) * NPAIR + c.p0) * 1024 + (w >> 2) * c.np * 512, min(lane, 4 * c.np - 1) * 128, lb + STAT);
  if (c.type == 1) {
    const char* p = ws + WS_PQ + (size_t)((4 * c.ti + (w & 3)) * NH + 16 * (c.jc - 10)) * 2048 + (w >> 2) * 16384;
    touchd(p, lane * 128, lb + STAT); touchd(p + 8192, lane * 128, lb + STAT);
  }
  if (w >= 4) stage2<true>(sm, lb, c, ws, w, lane, ti2, cj, x, wo, out);
  else stage2<false>(sm, lb, c, ws, w, lane, ti2, cj, x, wo, out);
}

extern "C" void kernel_launch(void* const* d_in, const int* in_sizes, int n_in, void* d_out, int out_size, void* d_ws,
                              size_t ws_size, hipStream_t stream) {
  fused<<<256, 512, 0, stream>>>((const u16*)d_in[0], (const u16*)d_in[1], (const u16*)d_in[2], (const u16*)d_in[3],
                                 (const u16*)d_in[5], (const u16*)d_in[6], (const u16*)d_in[7], (char*)d_ws, (u16*)d_out);
}
```

```cpp
#include <hip/hip_runtime.h>
#include <hip/hip_bf16.h>

typedef unsigned short u16;
typedef unsigned int u32;
typedef unsigned long long u64;
typedef short s16x4 __attribute__((ext_vector_type(4)));
typedef __bf16 bf16x8 __attribute__((ext_vector_type(8)));
typedef __bf16 bf16x2 __attribute__((ext_vector_type(2)));
typedef float f32x2 __attribute__((ext_vector_type(2)));
typedef float f32x4 __attribute__((ext_vector_type(4)));
typedef float f32x16 __attribute__((ext_vector_type(16)));
typedef u32 u32x2 __attribute__((ext_vector_type(2)));
typedef u32 u32x4 __attribute__((ext_vector_type(4)));
#define LAS __attribute__((address_space(3)))
#define DI __device__ __forceinline__
#define MFMA32(a, b, c) __builtin_amdgcn_mfma_f32_32x32x16_bf16((a), (b), (c), 0, 0, 0)
#define BF8(x) __builtin_bit_cast(bf16x8, (x))

#define MAP1 1
#define ST_CALC 4
#define ST_GATHER 12

constexpr int DM = 512, FF = 1376, NH = 32, NPAIR = 86;
constexpr size_t WS_H = 0;
constexpr size_t WS_PQ = WS_H + (size_t)64 * 88 * 1024;
constexpr size_t WS_KVP = WS_PQ + (size_t)64 * 32 * 2048;
constexpr size_t WS_KVF = WS_KVP + (size_t)16 * 32 * 4096;
constexpr size_t WS_RS = WS_KVF + 65536;
constexpr size_t WS_F1 = WS_RS + 32768;
constexpr size_t WS_F2 = WS_F1 + 16384;
constexpr size_t WS_EP = WS_F2 + 2048;
constexpr u32 SPIN_CAP = 1u << 15;

#define STAMP(s) do {} while (0)

template <int N> struct IC { static constexpr int value = N; };
template <int B, int E, class F> DI void sfor(F&& f) { if constexpr (B < E) { f(IC<B>{}); sfor<B + 1, E>(f); } }

DI void dma16(const void* sbase, u32 voff, u32 lds) {
  asm volatile("s_mov_b32 m0, %2\n\ts_nop 0\n\tglobal_load_lds_dwordx4 %0, %1" ::"v"(voff), "s"(sbase), "s"(__builtin_amdgcn_readfirstlane(lds)) : "memory");
}
DI void dma16d(const void* sbase, u32 voff, u32 lds) {
  asm volatile("s_mov_b32 m0, %2\n\ts_nop 0\n\tglobal_load_lds_dwordx4 %0, %1 sc1" ::"v"(voff), "s"(sbase), "s"(__builtin_amdgcn_readfirstlane(lds)) : "memory");
}
template <int IMM> DI void set_m0(u32 base) { asm volatile("s_add_u32 m0, %0, %1" ::"s"(base), "n"(IMM) : "memory", "scc"); }
template <int J> DI void dma_p(u32 voff, const void* sbase) {
  asm volatile("global_load_lds_dwordx4 %0, %1 offset:%2" ::"v"(voff), "s"(sbase), "n"(J * 1024) : "memory");
}
template <int J> DI void dma_pm(u32 voff, const void* sbase, u64 em) {
  asm volatile("s_mov_b64 exec, %2\n\tglobal_load_lds_dwordx4 %0, %1 offset:%3\n\ts_mov_b64 exec, -1" ::"v"(voff), "s"(sbase), "s"(em), "n"(J * 1024) : "memory");
}
template <int IMM, int STEP> DI void dma_ring(u32 base, u32& voff, const void* sbase) {
  asm volatile("s_add_u32 m0, %1, %3\n\tv_add_u32 %0, %4, %0\n\tglobal_load_lds_dwordx4 %0, %2 sc1" : "+v"(voff) : "s"(base), "s"(sbase), "n"(IMM), "n"(STEP) : "memory", "scc");
}
DI void dma_ring2(u32 voff, const void* sbase) { asm volatile("global_load_lds_dwordx4 %0, %1 offset:1024 sc1" ::"v"(voff), "s"(sbase) : "memory"); }
DI float max3(float a, float b, float c) { float d; asm("v_max3_f32 %0, %1, %2, %3" : "=v"(d) : "v"(a), "v"(b), "v"(c)); return d; }
DI float nmin3(float a, float b, float c) { float d; asm("v_max3_f32 %0, -%1, -%2, -%3" : "=v"(d) : "v"(a), "v"(b), "v"(c)); return d; }
DI float nmin3m(float m, float b, float c) { float d; asm("v_max3_f32 %0, %1, -%2, -%3" : "=v"(d) : "v"(m), "v"(b), "v"(c)); return d; }
template <int OFF> DI void st16o(void* sbase, u32 voff, const u32x4& v) {
  asm volatile("global_store_dwordx4 %0, %1, %2 offset:%3 sc1\n\ts_nop 1" ::"v"(voff), "v"(v), "s"(sbase), "n"(OFF) : "memory");
}
DI void touch(const void* p, u32 lds) {
  asm volatile("s_mov_b32 m0, %1\n\ts_nop 0\n\tglobal_load_lds_dword %0, off" ::"v"(p), "s"(__builtin_amdgcn_readfirstlane(lds)) : "memory");
}
DI void touchd(const void* sbase, u32 voff, u32 lds) {
  asm volatile("s_mov_b32 m0, %2\n\ts_nop 0\n\tglobal_load_lds_dword %0, %1 sc1" ::"v"(voff), "s"(sbase), "s"(__builtin_amdgcn_readfirstlane(lds)) : "memory");
}
template <int N> DI void lgw() { asm volatile("s_waitcnt lgkmcnt(%0)" ::"n"(N) : "memory"); }
DI f32x2 pkfma(f32x2 a, f32x2 b, f32x2 c) { return __builtin_elementwise_fma(a, b, c); }
DI f32x2 pr(const f32x16& v, int i) { return (f32x2){v[i], v[i + 1]}; }
DI f32x2 sp2(float a) { return (f32x2){a, a}; }
template <int N> DI void vmwait() { asm volatile("s_waitcnt vmcnt(%0)" ::"n"(N) : "memory"); }
DI void lgwait() { asm volatile("s_waitcnt lgkmcnt(0)" ::: "memory"); }
DI void bar() { asm volatile("s_barrier" ::: "memory"); }
DI void st16d(void* sbase, u32 voff, const u32x4& v) { asm volatile("global_store_dwordx4 %0, %1, %2 sc1\n\ts_nop 1" ::"v"(voff), "v"(v), "s"(sbase) : "memory"); }
DI void st8d(void* sbase, u32 voff, const u32x2& v) { asm volatile("global_store_dwordx2 %0, %1, %2 sc1\n\ts_nop 1" ::"v"(voff), "v"(v), "s"(sbase) : "memory"); }
DI void st4d(void* sbase, u32 voff, u32 v) { asm volatile("global_store_dword %0, %1, %2 sc1\n\ts_nop 1" ::"v"(voff), "v"(v), "s"(sbase) : "memory"); }
DI void ld16(u32x4& d, const void* sbase, u32 voff) { asm volatile("global_load_dwordx4 %0, %1, %2" : "+v"(d) : "v"(voff), "s"(sbase) : "memory"); }
DI void ld16d(u32x4& d, const void* sbase, u32 voff) { asm volatile("global_load_dwordx4 %0, %1, %2 sc1" : "+v"(d) : "v"(voff), "s"(sbase) : "memory"); }
DI void ld8d(u32x2& d, const void* sbase, u32 voff) { asm volatile("global_load_dwordx2 %0, %1, %2 sc1" : "=v"(d) : "v"(voff), "s"(sbase) : "memory"); }
template <int OFF> DI void trd(u32x2& d, u32 a) { asm volatile("ds_read_b64_tr_b16 %0, %1 offset:%2" : "=v"(d) : "v"(a), "n"(OFF) : "memory"); }
template <int OFF> DI void rd128(u32x4& d, u32 a) { asm volatile("ds_read_b128 %0, %1 offset:%2" : "=v"(d) : "v"(a), "n"(OFF) : "memory"); }
DI u32x4 cat(const u32x2& a, const u32x2& b) { return (u32x4){a[0], a[1], b[0], b[1]}; }
DI void amfma(f32x16& c, const u32x4& a, const u32x4& b) { asm volatile("v_mfma_f32_32x32x16_bf16 %0, %1, %2, %0" : "+v"(c) : "v"(a), "v"(b)); }
DI void amfma16(f32x4& c, const u32x4& a, const u32x4& b) { asm volatile("v_mfma_f32_16x16x32_bf16 %0, %1, %2, %0" : "+v"(c) : "v"(a), "v"(b)); }
DI void amfma0(f32x16& c, const u32x4& a, const u32x4& b) { asm volatile("v_mfma_f32_32x32x16_bf16 %0, %1, %2, 0" : "=&v"(c) : "v"(a), "v"(b)); }
DI void amfma_n(f32x16& c, const u32x4& a, const u32x4& b) { asm volatile("s_nop 1\n\tv_mfma_f32_32x32x16_bf16 %0, %1, %2, %0" : "+v"(c) : "v"(a), "v"(b)); }

DI bf16x8 tr_frag(LAS char* p0, LAS char* p1) {
  s16x4 a = __builtin_amdgcn_ds_read_tr16_b64_v4i16((LAS s16x4*)p0);
  s16x4 b = __builtin_amdgcn_ds_read_tr16_b64_v4i16((LAS s16x4*)p1);
  return BF8(__builtin_shufflevector(a, b, 0, 1, 2, 3, 4, 5, 6, 7));
}
DI u32 pk2(float a, float b) { f32x2 v = {a, b}; return __builtin_bit_cast(u32, __builtin_convertvector(v, bf16x2)); }
DI u32 pkv(f32x2 v) { return __builtin_bit_cast(u32, __builtin_convertvector(v, bf16x2)); }
DI float bfr(float a) { return __uint_as_float(pk2(a, a) & 0xffff0000u); }
DI float lo_f(u32 w) { return __uint_as_float(w << 16); }
DI float hi_f(u32 w) { return __uint_as_float(w & 0xffff0000u); }
DI float fexp2(float x) { return __builtin_amdgcn_exp2f(x); }
DI float frcp(float x) { return __builtin_amdgcn_rcpf(x); }
DI void swap32(float& a, float& b) {
  auto r = __builtin_amdgcn_permlane32_swap(__float_as_uint(a), __float_as_uint(b), false, false);
  a = __uint_as_float(r[0]); b = __uint_as_float(r[1]);
}
template <int C> DI float dpp_add(float a) { return a + __uint_as_float(__builtin_amdgcn_update_dpp(0, __float_as_uint(a), C, 0xf, 0xf, true)); }
DI float wave_sum(float a) {
  a = dpp_add<0xB1>(a); a = dpp_add<0x4E>(a); a = dpp_add<0x141>(a); a = dpp_add<0x140>(a);
  auto r = __builtin_amdgcn_permlane16_swap(__float_as_uint(a), __float_as_uint(a), false, false);
  a = __uint_as_float(r[0]) + __uint_as_float(r[1]);
  float b = a; swap32(a, b);
  return a + b;
}
DI void poll8(const void* sbase, u32 voff, u32x2 tag) {
#pragma unroll 1
  for (u32 spin = 0; spin < SPIN_CAP; spin++) {
    u32x2 v;
    asm volatile("global_load_dwordx2 %0, %1, %2 sc1\n\ts_waitcnt vmcnt(0)" : "=&v"(v) : "v"(voff), "s"(sbase) : "memory");
    if (__all(v[0] == tag[0] && v[1] == tag[1])) break;
    __builtin_amdgcn_s_sleep(2);
  }
}

DI void ld_f2(u32x4& a, u32x4& b, const void* sbase, u32 voff) {
  asm volatile("global_load_dwordx4 %0, %2, %3 sc1\n\tglobal_load_dwordx4 %1, %2, %3 offset:1024 sc1" : "=&v"(a), "=&v"(b) : "v"(voff), "s"(sbase) : "memory");
}
DI void ld_f2_w0(u32x4& a, u32x4& b, const void* sbase, u32 voff, int w) {
  asm volatile("s_cmp_lg_u32 %4, 0\n\ts_cbranch_scc1 .Lf2%=\n\tglobal_load_dwordx4 %0, %2, %3 sc1\n\tglobal_load_dwordx4 %1, %2, %3 offset:1024 sc1\n.Lf2%=:"
               : "=&v"(a), "=&v"(b) : "v"(voff), "s"(sbase), "s"(w) : "memory", "scc");
}
DI void poll_f2(u32x4& a, u32x4& b, const void* sbase, u32 voff, u32x2 tag) {
  vmwait<0>();
  asm volatile("" : "+v"(a), "+v"(b));
#pragma unroll 1
  for (u32 spin = 0; spin < SPIN_CAP; spin++) {
    const u32 d = ((a[0] ^ tag[0]) | (a[1] ^ tag[1]) | (a[2] ^ tag[0])) | ((a[3] ^ tag[1]) | (b[0] ^ tag[0]) | (b[1] ^ tag[1])) | ((b[2] ^ tag[0]) | (b[3] ^ tag[1]));
    if (__all(d == 0)) break;
    __builtin_amdgcn_s_sleep(2);
    ld_f2(a, b, sbase, voff);
    vmwait<0>();
    asm volatile("" : "+v"(a), "+v"(b));
  }
}

constexpr int SEG1 = 9344, WSLOT = 18688, XRING = 6 * WSLOT, XSLOT = 16384, STAT = XRING + 3 * XSLOT;
constexpr int WPHI_L = XRING + 2 * XSLOT;
constexpr int VIMG_L = XRING, ONES_L = STAT + 1152;
DI constexpr int phik_l(int t) { return t < 6 ? WSLOT + t * 8192 : WPHI_L + (t - 6) * 8192; }
DI constexpr int piece_l(int n) {
  return n < 72 ? ((n / 18 + 4) % 6) * WSLOT + ((n % 18) / 9) * SEG1 + (n % 9) * 1024
       : n < 88 ? XRING + (n - 72) * 1024 : n < 96 ? WPHI_L + 8192 + (n - 88) * 1024
       : n < 132 ? 2 * WSLOT + (n - 96) * 1024 : XRING + XSLOT + (n - 132) * 1024;
}
constexpr int SLOT2 = 12288, WO_L = 8 * SLOT2, REST_L = 6 * SLOT2, LDSB = 163840;
constexpr int R1F = 38;

struct Ctx {
  const u16* seg0; const u16* seg1; const u16* xt; const u16* wphi; const char* wdc; const char *wqkv, *wg, *wu, *wd, *wo;
  int wpo;
  int ld, np, type, ti, jc, p0;
  u32x2 tag;
};

template <bool NORM>
DI void dual_softmax(const f32x16& p, float sc, u32x4 (&fp)[2], u32x4 (&fm)[2]) {
  float mx[2], nn[2], sp[2], sm[2];
  f32x2 ep[2][4], em[2][4];
#pragma unroll
  for (int e = 0; e < 2; e++) {
    const int o = 8 * e;
    mx[e] = max3(max3(max3(p[o], p[o + 1], p[o + 2]), p[o + 3], p[o + 4]), max3(p[o + 5], p[o + 6], p[o + 7]), p[o]);
    nn[e] = nmin3m(nmin3m(nmin3m(nmin3(p[o], p[o + 1], p[o + 2]), p[o + 3], p[o + 4]), p[o + 5], p[o + 6]), p[o + 7], p[o]);
  }
#pragma unroll
  for (int e = 0; e < 2; e++) swap32(mx[e], nn[e]);
#pragma unroll
  for (int e = 0; e < 2; e++) { mx[e] = max3(mx[e], nn[e], nn[e]) * -sc; nn[e] = mx[e]; }
#pragma unroll
  for (int e = 0; e < 2; e++) swap32(mx[e], nn[e]);
#pragma unroll
  for (int e = 0; e < 2; e++)
#pragma unroll
    for (int j = 0; j < 4; j++) { ep[e][j] = pkfma(pr(p, 8 * e + 2 * j), sp2(sc), sp2(mx[e])); em[e][j] = pkfma(pr(p, 8 * e + 2 * j), sp2(-sc), sp2(nn[e])); }
#pragma unroll
  for (int e = 0; e < 2; e++)
#pragma unroll
    for (int j = 0; j < 4; j++) {
      ep[e][j] = (f32x2){fexp2(ep[e][j][0]), fexp2(ep[e][j][1])};
      em[e][j] = (f32x2){fexp2(em[e][j][0]), fexp2(em[e][j][1])};
    }
#pragma unroll
  for (int e = 0; e < 2; e++) {
    const f32x2 tp = (ep[e][0] + ep[e][1]) + (ep[e][2] + ep[e][3]), tm = (em[e][0] + em[e][1]) + (em[e][2] + em[e][3]);
    sp[e] = tp[0] + tp[1]; sm[e] = tm[0] + tm[1];
  }
#pragma unroll
  for (int e = 0; e < 2; e++) swap32(sp[e], sm[e]);
#pragma unroll
  for (int e = 0; e < 2; e++) { sp[e] += sm[e]; if (NORM) sp[e] = frcp(sp[e]); sm[e] = sp[e]; }
#pragma unroll
  for (int e = 0; e < 2; e++) swap32(sp[e], sm[e]);
#pragma unroll
  for (int e = 0; e < 2; e++) {
    if (!NORM) sm[e] = sp[e] * frcp(sm[e]);
#pragma unroll
    for (int j = 0; j < 4; j++) {
      if (NORM) ep[e][j] *= sp2(sp[e]);
      em[e][j] *= sp2(sm[e]);
      fp[e][j] = pkv(ep[e][j]); fm[e][j] = pkv(em[e][j]);
    }
  }
}

template <int NT, bool XR>
DI void mainloop(u32 lb, Ctx& c, char* ws, int w, int lane, f32x16 (&acc)[NT], f32x4& cs0, f32x4& cs1, u32x4& gran) {
  const int tg = w & 3, pg = w >> 2;
  const int r = lane & 31, h = lane >> 5, g16 = (lane >> 4) & 1, q4 = (lane & 15) >> 2, pp = lane & 3;
  const int T0 = pg ? (c.np == 9 ? 5 : 4) : 0;
  const u32 vlin = lane * 16;
  const u32 vw = (lane >> 3) * 1024 + 16 * ((lane & 7) ^ (((lane >> 4) & 1) << 2));
  u32 voff[4];
  const char* src;
  u32 lds, sstep = 0;
  if constexpr (!XR) {
    const int seg = w >= 3, m0 = 3 * (w - 3 * seg), emax = 2 * c.np - 1;
#pragma unroll
    for (int m = 0; m < 3; m++) {
      const int ch = 64 * (m0 + m) + lane, row = (ch * 3641) >> 16;
      int e = ch - row * 18; e = e > emax ? emax : e;
      voff[m] = (u32)(row * c.ld + 8 * e) * 2u - m * 1024;
    }
    voff[3] = 0;
    src = (const char*)(seg ? c.seg1 : c.seg0);
    lds = lb + seg * SEG1 + m0 * 1024;
    sstep = 64 * c.ld;
  } else {
    const int rr = lane >> 3, pos = lane & 7, f = lane >> 4;
    voff[0] = rr * 1024 + 16 * (pos ^ f);
    voff[1] = rr * 1024 + 16 * (pos ^ f ^ 4) + 7168;
    voff[2] = voff[0] + 14336; voff[3] = voff[1] + 14336;
    src = (const char*)c.xt + (w - 6) * 65536;
    lds = lb + XRING + (w - 6) * 8192;
  }
  lds = __builtin_amdgcn_readfirstlane(lds);
  const u64 em = c.type == 2 ? 0ull : ~0ull;
  const char* cur = src;
  auto setup = [&](auto u_) {
    constexpr int u = decltype(u_)::value;
    if constexpr (!XR) {
      constexpr int U = u + 4;
      if constexpr (U == 16) {
        src = c.wdc + 3 * w * 8192; sstep = 18 * 8192;
        voff[0] = vw; voff[1] = vw + 7168; voff[2] = vw + 14336;
      }
      if constexpr (U <= 19) { set_m0<(U % 6) * WSLOT>(lds); cur = src; src += sstep; }
    } else {
      constexpr int sl = (u + 3) >> 1, hf = (u + 3) & 1;
      if constexpr (u == 13) {
        const bool ph = w == 6;
        src = ph ? (const char*)c.wphi : c.wdc + 88 * 8192; sstep = ph ? 4096 : 32768;
#pragma unroll
        for (int j = 0; j < 4; j++) voff[j] = ph ? vlin : vw + j * 7168;
      }
      if constexpr (u == 15) {
        src = c.wdc + (72 + (w - 6) * 8) * 8192; sstep = 32768;
#pragma unroll
        for (int j = 0; j < 4; j++) voff[j] = vw + j * 7168;
      }
      set_m0<(sl % 3) * XSLOT + hf * 4096>(lds);
      if constexpr (u < 13) cur = src + (sl * 128 + hf * 32768);
      else cur = hf ? src + sstep : src;
    }
  };
  auto piece = [&](auto u_, auto j_) {
    constexpr int u = decltype(u_)::value, j = decltype(j_)::value;
    if constexpr (XR ? u <= 16 : (u <= 15 && j < 3)) {
      if constexpr (u >= 15) dma_pm<j>(voff[j], cur, em); else dma_p<j>(voff[j], cur);
    }
  };
  auto unit = [&](auto u_) { setup(u_); asm volatile("s_nop 0"); sfor<0, 4>([&](auto j_) { piece(u_, j_); }); };
  u64 ep;
  asm volatile("s_load_dwordx2 %0, %1, 0x0" : "=s"(ep) : "s"(ws + WS_EP) : "memory");
  u32x4 srow;
  unit(IC<(XR ? -3 : -4)>{});
  asm volatile("global_load_dwordx4 %0, %1, %2" : "=v"(srow) : "v"(vlin), "s"(c.xt + (8 * c.jc + w) * DM) : "memory");
  sfor<(XR ? -2 : -3), 0>(unit);
  const char* wp = nullptr;
  u32 wstep = 0;
  if constexpr (NT == 4 && !XR) {
    const int b = blockIdx.x;
    if (w == 5) {
      const int g = b * 59 + min(lane, 58);
      wp = g < 11008 ? c.wd + g * 128 : c.wo + (g - 11008) * 128;
    } else {
      const int g = min(b * 34 + min(lane, 33), 8575);
      wstep = g < 3072 ? 393216 : 352256;
      wp = (g < 3072 ? c.wqkv + g * 128 : g < 5824 ? c.wg + (g - 3072) * 128 : c.wu + (g - 5824) * 128) + wstep;
      if (w == 4) touch(wp, lb + STAT + 1280);
    }
  }
  u32 wl[NT], wh[NT], xa[4];
#pragma unroll
  for (int s = 0; s < NT; s++) {
    int t = s + tg; t = t >= NT ? t - NT : t;
    wl[s] = lb + g16 * SEG1 + (8 * h + q4) * 288 + 8 * pp + 32 * (T0 + t);
    wh[s] = wl[s] + 3 * WSLOT;
#pragma unroll
    for (int j = 0; j < 16; j++) acc[s][j] = 0.f;
    asm volatile("" : "+v"(acc[s]), "+v"(wl[s]), "+v"(wh[s]));
  }
#pragma unroll
  for (int k = 0; k < 4; k++) { xa[k] = (lb + XRING + (32 * tg + r) * 128 + 16 * (h ^ ((r >> 1) & 7))) ^ (32 * k); asm volatile("" : "+v"(xa[k])); }
  cs0 = (f32x4){0.f, 0.f, 0.f, 0.f}; cs1 = cs0;
  const u32 one = ((lane & 31) == 0 || (lane & 31) == 17) ? 0x3f803f80u : 0u;
  u32x4 sel = {one, one, one, one};
  u32 gaddr = r * 16, vw_ = vw;
  asm volatile("s_waitcnt lgkmcnt(0)" : "+s"(ep), "+v"(cs0), "+v"(cs1), "+v"(sel), "+v"(gaddr), "+v"(vw_), "+v"(wp), "+v"(wstep) :: "memory");
  ep += 1;
  c.tag = (u32x2){(u32)ep, (u32)(ep >> 32)};
  u32x2 A[2][NT][2];
  u32x4 B[2];
  auto rdw = [&](auto k_, auto s_) {
    constexpr int k = decltype(k_)::value, s = decltype(s_)::value, sl = (k >> 1) % 6, O = (sl % 3) * WSLOT + (k & 1) * 4608;
    trd<O>(A[k & 1][s][0], sl < 3 ? wl[s] : wh[s]); trd<O + 1152>(A[k & 1][s][1], sl < 3 ? wl[s] : wh[s]);
  };
  auto rdx = [&](auto k_) { constexpr int k = decltype(k_)::value; rd128<((k >> 2) % 3) * XSLOT>(B[k & 1], xa[k & 3]); };
  if constexpr (XR) vmwait<4>(); else vmwait<9>();
  bar();
  STAMP(1);
  rdx(IC<0>{});
  sfor<0, NT>([&](auto s_) { rdw(IC<0>{}, s_); });
  setup(IC<0>{}); asm volatile("s_nop 0"); piece(IC<0>{}, IC<0>{}); piece(IC<0>{}, IC<1>{});
  sfor<0, 32>([&](auto k_) {
    constexpr int k = decltype(k_)::value, C = k & 1;
    constexpr int u = (k + 1) >> 1;
    constexpr bool N = k < 31;
    using K1 = IC<k + 1>;
    lgwait();
    amfma(acc[0], cat(A[C][0][0], A[C][0][1]), B[C]);
    if constexpr (C && u < 16) {
      if constexpr (XR) { if constexpr (!(u & 1)) vmwait<4>(); } else vmwait<9>();
      bar();
    }
    if constexpr (k == 31 && XR) { vmwait<4>(); if (c.type == 2) vmwait<0>(); }
    if constexpr (k == 31) bar();
    if constexpr (C) {
      if constexpr (NT == 4 && !XR && (u == 1 || u == 3)) { if (w == 4) { wp += wstep; touch(wp, lb + STAT + 1280); } }
      if constexpr (NT == 4 && !XR && u == 7) { if (w == 5) touch(wp, lb + STAT + 1280); }
      if constexpr (u == ST_GATHER) asm volatile("global_load_dwordx4 %0, %1, %2 sc1" : "=v"(gran) : "v"(gaddr), "s"(ws + WS_RS + (128 * c.ti + 32 * tg) * 16) : "memory");
      setup(IC<u>{});
    }
    if constexpr (N) { rdx(K1{}); rdw(K1{}, IC<0>{}); }
    piece(IC<u>{}, IC<(C ? 0 : 2)>{});
    amfma(acc[1], cat(A[C][1][0], A[C][1][1]), B[C]);
    if constexpr (N) { rdw(K1{}, IC<1>{}); rdw(K1{}, IC<2>{}); }
    amfma16(cs0, sel, cat(A[C][0][0], A[C][0][1]));
    amfma(acc[2], cat(A[C][2][0], A[C][2][1]), B[C]);
    if constexpr (N) { rdw(K1{}, IC<3>{}); if constexpr (NT == 5) rdw(K1{}, IC<NT - 1>{}); }
    piece(IC<u>{}, IC<(C ? 1 : 3)>{});
    amfma(acc[3], cat(A[C][3][0], A[C][3][1]), B[C]);
    if constexpr (NT == 5) {
      amfma16(cs1, sel, cat(A[C][1][0], A[C][1][1]));
      amfma(acc[NT - 1], cat(A[C][NT - 1][0], A[C][NT - 1][1]), B[C]);
    }
    if constexpr (k == 2 * ST_CALC - 1) {
      asm volatile("" : "+v"(srow));
      const float piv = lo_f(__builtin_amdgcn_readfirstlane(srow[0]));
      f32x2 v[4];
#pragma unroll
      for (int j = 0; j < 4; j++) v[j] = (f32x2){lo_f(srow[j]), hi_f(srow[j])} - sp2(piv);
      const f32x2 s2 = (v[0] + v[1]) + (v[2] + v[3]), q2 = pkfma(v[0], v[0], v[1] * v[1]) + pkfma(v[2], v[2], v[3] * v[3]);
      const float ms = wave_sum(s2[0] + s2[1]) * (1.f / 512.f), mq = wave_sum(q2[0] + q2[1]) * (1.f / 512.f);
      const float mean = piv + ms, var = fmaxf(mq - ms * ms, 0.f);
      const float sd = bfr(__builtin_amdgcn_sqrtf(bfr(bfr(var) + 1e-5f)));
      st16d(ws + WS_RS + (128 * c.ti + 8 * c.jc + w) * 16, 0u, (u32x4){__float_as_uint(sd), __float_as_uint(bfr(mean)), c.tag[0], c.tag[1]});
    }
  });
  piece(IC<16>{}, IC<2>{}); piece(IC<16>{}, IC<3>{});
  if constexpr (XR) vmwait<4 * (17 - ST_GATHER)>();
  else { if (c.type == 2) vmwait<3 * (15 - ST_GATHER)>(); else vmwait<3 * (16 - ST_GATHER)>(); }
  asm volatile("s_nop 15" : "+v"(cs0), "+v"(cs1), "+v"(gran) :: "memory");
#pragma unroll
  for (int s = 0; s < NT; s++) asm volatile("" : "+v"(acc[s]));
}

template <int NT>
DI void stage1(LAS char* sm, u32 lb, Ctx& c, char* ws, int w, int lane) {
  const int tg = w & 3, pg = w >> 2;
  const int r = lane & 31, h = lane >> 5, g16 = (lane >> 4) & 1, q4 = (lane & 15) >> 2, pp = lane & 3;
  const int T0 = pg ? (c.np == 9 ? 5 : 4) : 0;
  const u32 vlin = lane * 16;
  const u32 vw = (lane >> 3) * 1024 + 16 * ((lane & 7) ^ (((lane >> 4) & 1) << 2));
  f32x16 acc[NT];
  f32x4 cs0, cs1;
  u32x4 gran;
  if (NT == 4 && w >= 6) mainloop<NT, true>(lb, c, ws, w, lane, acc, cs0, cs1, gran);
  else mainloop<NT, false>(lb, c, ws, w, lane, acc, cs0, cs1, gran);
  STAMP(2);
  if (lane < 16) {
    LAS float* cs = (LAS float*)(sm + STAT);
    cs[(T0 + tg) * 32 + lane] = cs0[0]; cs[(T0 + tg) * 32 + 16 + lane] = cs0[1];
    if (NT == 5 && tg == 3) { cs[128 + lane] = cs1[0]; cs[144 + lane] = cs1[1]; }
  }
  u32x4 wab[4];
  if (NT == 4 && c.type) {
    LAS char* wp = sm + WPHI_L + c.wpo + (4 * h + q4) * 32 + 8 * pp;
#pragma unroll
    for (int s = 0; s < 4; s++) {
      const int t = c.type == 1 ? T0 + ((s + tg) & 3) + 8 * g16 : T0 + (((s & 2) + g16 + tg) & 3);
      wab[s] = __builtin_bit_cast(u32x4, tr_frag(wp + t * 512, wp + t * 512 + 256));
    }
  }
#pragma unroll 1
  for (u32 spin = 0; spin < SPIN_CAP && !__all(gran[2] == c.tag[0] && gran[3] == c.tag[1]); spin++) {
    __builtin_amdgcn_s_sleep(2);
    asm volatile("global_load_dwordx4 %0, %1, %2 sc1\n\ts_waitcnt vmcnt(0)" : "=&v"(gran) : "v"(r * 16), "s"(ws + WS_RS + (128 * c.ti + 32 * tg) * 16) : "memory");
  }
  const float sd = __uint_as_float(gran[0]), nmean = -__uint_as_float(gran[1]), rs = 1.f / sd;
  lgwait();
  bar();
  auto w2 = [&](int j) { const int n = 96 + w + 8 * j; dma16(c.wdc + n * 8192, vw, lb + piece_l(n)); };
#pragma unroll
  for (int s = 0; s < NT; s++) {
    int t = s + tg; t = t >= NT ? t - NT : t;
    LAS f32x4* cp = (LAS f32x4*)(sm + STAT + ((T0 + t) * 32 + 4 * h) * 4);
#pragma unroll
    for (int i = 0; i < 4; i++) {
      const f32x4 cv = cp[2 * i];
      const f32x2 a0 = pkfma(sp2(nmean), (f32x2){cv[0], cv[1]}, pr(acc[s], 4 * i)), a1 = pkfma(sp2(nmean), (f32x2){cv[2], cv[3]}, pr(acc[s], 4 * i + 2));
      acc[s][4 * i] = a0[0]; acc[s][4 * i + 1] = a0[1]; acc[s][4 * i + 2] = a1[0]; acc[s][4 * i + 3] = a1[1];
    }
  }
  const int tg32 = 4 * c.ti + tg;
  if (c.type == 0) {
    const float K0 = -2.f * 0.7978845608028654f * 1.4426950408889634f * rs, K1 = K0 * 0.044715f * rs * rs, v2 = sd * sd;
    char* dst = ws + WS_H + (size_t)(tg32 * NPAIR + c.p0 + T0) * 1024;
#pragma unroll
    for (int s = 0; s < NT; s++) {
      int t = s + tg; t = t >= NT ? t - NT : t;
      if (s < 4) { w2(2 * s); if (s < 3) w2(2 * s + 1); }
      f32x2 e[4], gu[4];
#pragma unroll
      for (int j = 0; j < 4; j++) { const f32x2 g = pr(acc[s], 2 * j); e[j] = g * pkfma(g * g, sp2(K1), sp2(K0)); gu[j] = g * pr(acc[s], 8 + 2 * j); }
#pragma unroll
      for (int j = 0; j < 4; j++) e[j] = pkfma((f32x2){fexp2(e[j][0]), fexp2(e[j][1])}, sp2(v2), sp2(v2));
#pragma unroll
      for (int j = 0; j < 4; j++) gu[j] *= (f32x2){frcp(e[j][0]), frcp(e[j][1])};
      st16d(dst, vlin + t * 1024, (u32x4){pkv(gu[0]), pkv(gu[1]), pkv(gu[2]), pkv(gu[3])});
    }
  } else if constexpr (NT == 4) {
    const float sc = rs * 1.4426950408889634f;
    f32x16 zero;
#pragma unroll
    for (int j = 0; j < 16; j++) zero[j] = 0.f;
    if (c.type == 1) {
      char* dst = ws + WS_PQ + (size_t)(tg32 * NH + 16 * (c.jc - 10) + T0) * 2048;
      f32x16 P[4];
#pragma unroll
      for (int s = 0; s < 4; s++) {
        u32x4 wA, wB, qa, qb;
#pragma unroll
        for (int j = 0; j < 4; j++) {
          wA[j] = g16 ? 0u : wab[s][j]; wB[j] = g16 ? wab[s][j] : 0u;
          qa[j] = pkv(pr(acc[s], 2 * j)); qb[j] = pkv(pr(acc[s], 8 + 2 * j));
        }
        P[s] = MFMA32(BF8(wA), BF8(qa), zero);
        P[s] = MFMA32(BF8(wB), BF8(qb), P[s]);
      }
#pragma unroll
      for (int s = 0; s < 4; s++) {
        const u32 vo = vlin + ((s + tg) & 3) * 2048;
        w2(2 * s); if (s < 3) w2(2 * s + 1);
        u32x4 fp[2], fm[2];
        dual_softmax<false>(P[s], sc, fp, fm);
        st16o<0>(dst, vo, fp[0]); st16o<1024>(dst, vo, fm[0]);
        st16o<0>(dst + 16384, vo, fp[1]); st16o<1024>(dst + 16384, vo, fm[1]);
      }
    } else {
      const int tok = 32 * tg + r;
      const u32 pw = tok * 64, vwl = VIMG_L + tok * 32;
      const int fsw = (r >> 1) & 7, vsw = (r >> 2) & 3;
      f32x16 P[2];
#pragma unroll
      for (int s = 0; s < 4; s += 2) {
        u32x4 wA, wB, ka, kb;
#pragma unroll
        for (int j = 0; j < 4; j++) {
          wA[j] = g16 ? 0u : wab[s][j]; wB[j] = g16 ? wab[s][j] : 0u;
          ka[j] = pkv(pr(acc[s], 2 * j)); kb[j] = pkv(pr(acc[s + 1], 2 * j));
        }
        P[s >> 1] = MFMA32(BF8(wA), BF8(ka), zero);
        P[s >> 1] = MFMA32(BF8(wB), BF8(kb), P[s >> 1]);
      }
#pragma unroll
      for (int s = 0; s < 4; s++) {
        LAS char* vd = sm + vwl + (T0 + ((s + tg) & 3)) * 4096;
        const f32x16& a = acc[s];
        *(LAS u32x2*)(vd + 8 * ((0 + h) ^ vsw)) = (u32x2){pkv(pr(a, 8) * sp2(rs)), pkv(pr(a, 10) * sp2(rs))};
        *(LAS u32x2*)(vd + 8 * ((2 + h) ^ vsw)) = (u32x2){pkv(pr(a, 12) * sp2(rs)), pkv(pr(a, 14) * sp2(rs))};
      }
#pragma unroll
      for (int s = 0; s < 4; s += 2) {
        u32x4 fp[2], fm[2];
        dual_softmax<true>(P[s >> 1], sc, fp, fm);
#pragma unroll
        for (int e = 0; e < 2; e++) {
          LAS char* pd = sm + pw + phik_l(T0 + ((s + e + tg) & 3));
          *(LAS u32x2*)(pd + 8 * ((0 + h) ^ fsw)) = (u32x2){fp[e][0], fp[e][1]};
          *(LAS u32x2*)(pd + 8 * ((2 + h) ^ fsw)) = (u32x2){fp[e][2], fp[e][3]};
          *(LAS u32x2*)(pd + 8 * ((4 + h) ^ fsw)) = (u32x2){fm[e][0], fm[e][1]};
          *(LAS u32x2*)(pd + 8 * ((6 + h) ^ fsw)) = (u32x2){fm[e][2], fm[e][3]};
        }
      }
      STAMP(12);
      lgwait();
      bar();
      f32x16 kv = zero;
      {
        const int t0 = 8 * h + q4;
        const u32 pa0 = lb + phik_l(w) + t0 * 64 + 8 * ((4 * g16 + pp) ^ (t0 >> 1)), pa1 = lb + phik_l(w) + (t0 + 4) * 64 + 8 * ((4 * g16 + pp) ^ ((t0 >> 1) + 2));
        const u32 pb0 = lb + VIMG_L + w * 4096 + t0 * 32 + 8 * (pp ^ (2 * h)), pb1 = lb + VIMG_L + w * 4096 + (t0 + 4) * 32 + 8 * (pp ^ (2 * h + 1));
        u32x2 fa[8][2], fb[8][2];
        sfor<0, 8>([&](auto k_) {
          constexpr int k = decltype(k_)::value;
          trd<k * 1024>(fa[k][0], pa0); trd<k * 1024>(fa[k][1], pa1); trd<k * 512>(fb[k][0], pb0); trd<k * 512>(fb[k][1], pb1);
        });
        lgwait();
        sfor<0, 8>([&](auto k_) {
          constexpr int k = decltype(k_)::value;
          asm volatile("" : "+v"(fa[k][0]), "+v"(fa[k][1]), "+v"(fb[k][0]), "+v"(fb[k][1]));
          u32x4 bq = cat(fb[k][0], fb[k][1]);
#pragma unroll
          for (int j = 0; j < 4; j++) bq[j] = g16 ? 0x3f803f80u : bq[j];
          kv = MFMA32(BF8(cat(fa[k][0], fa[k][1])), BF8(bq), kv);
        });
      }
      if (r <= 16) {
        char* dst = ws + WS_KVP + (size_t)(c.ti * NH + 8 * (c.jc - 12) + w) * 4096;
#pragma unroll
        for (int g = 0; g < 4; g++)
          st16d(dst + g * 1024, vlin, (u32x4){__float_as_uint(kv[4 * g]), __float_as_uint(kv[4 * g + 1]), __float_as_uint(kv[4 * g + 2]), __float_as_uint(kv[4 * g + 3])});
      }
      STAMP(13);
      vmwait<0>();
      lgwait();
      bar();
      if (w == 0) st8d(ws + WS_F1 + (c.ti * 16 + c.jc) * 64, 0u, c.tag);
      STAMP(14);
#pragma unroll 1
      for (int n = 54 + w; n < 152; n += 8) dma16(c.wdc + n * 8192, vw, lb + piece_l(n));
    }
  }
}

constexpr int ops_after(bool lo, int t) {
  if (t < 0) return lo ? 12 : 6;
  int n = 0;
  for (int st = (t == 0 ? 6 : t + 7); st <= t + 7; st++) {
    if (st <= 14) n += lo ? 2 : 1;
    else if (st <= 30) n += lo ? 0 : 2;
  }
  if (lo && t == 12) n += 5;
  if (lo && t > 12 && t + 7 <= 30) n += 1;
  return n;
}
constexpr int need_n(bool lo, int t) {
  int last = 0;
  int tail = 0;
  if (t <= 5) { last = -1; tail = (5 - t) * (lo ? 2 : 1); }
  else if (t == 6) { last = 0; tail = lo ? 2 : 1; }
  else if (lo && t >= 15 && t <= 19) { last = 12; tail = 19 - t; }
  else last = t - 7;
  int n = tail;
  for (int k = last + 1; k < t; k++) n += ops_after(lo, k);
  return n;
}

template <bool HI>
DI void stage2(LAS char* sm, u32 lb, const Ctx& c, char* ws, int w, int lane, int ti2, int cj, const u16* x, const u16* wo, u16* out) {
  const int tg = w & 1, cg = (w >> 1) & 1, b = blockIdx.x;
  constexpr int kh = HI;
  const int r = lane & 31, h = lane >> 5, g16 = (lane >> 4) & 1, q4 = (lane & 15) >> 2, pp = lane & 3;
  const u32 vlin = lane * 16;
  const u32 vw = (lane >> 3) * 1024 + 16 * ((lane & 7) ^ (((lane >> 4) & 1) << 2));
  const u32 wa0 = lb + (4 * h + q4) * 128 + ((cg ^ (q4 >> 1)) << 6) + 32 * g16 + 8 * pp, wa = wa0 + kh * 2048;
  const int khead = b >> 3, kg = (b >> 1) & 3, khalf = b & 1;
  u32x2 part[16];
  if (HI && w == 7) {
    poll8(ws + WS_F1 + (12 + (khead >> 3)) * 64, (lane & 15) * 1024, c.tag);
    const char* src = ws + WS_KVP + khead * 4096 + kg * 1024 + khalf * 8;
    const u32 vo = (r > 16 ? (lane & 32) + 16 : lane) * 16;
#pragma unroll
    for (int i = 0; i < 16; i++) ld8d(part[i], src + (size_t)i * NH * 4096, vo);
  }
  u32x2 wf[43][2];
  auto readout = [&](auto k_) {
    sfor<0, R1F>([&](auto i) {
      constexpr int I = decltype(i)::value, n = 4 * I + 2 * decltype(k_)::value, O0 = piece_l(n), O1 = piece_l(n + 1);
      trd<(O0 & 0xffff)>(wf[I][0], wa0 + (O0 & ~0xffff)); trd<(O1 & 0xffff)>(wf[I][1], wa0 + (O1 & ~0xffff));
    });
    lgwait();
  };
  readout(IC<kh>{});
  if (HI && w == 7) {
    vmwait<0>();
    float s0 = 0.f, s1 = 0.f;
#pragma unroll
    for (int i = 0; i < 16; i++) { asm volatile("" : "+v"(part[i])); s0 += __uint_as_float(part[i][0]); s1 += __uint_as_float(part[i][1]); }
    if (r >= 16 && kg < 2) { s0 += 1e-6f; s1 += 1e-6f; }
    st4d(ws + WS_KVF + khead * 2048 + (kg >> 1) * 1024 + (kg & 1) * 8 + khalf * 4, vlin, pk2(s0, s1));
  }
  lgwait();
  bar();
  STAMP(5);
  if (HI || w) {
#pragma unroll 1
    for (int m = 0; m < 12; m++) {
      const int n = w - 1 + 7 * m;
      if (n < 20) dma16(c.wdc + (152 + n) * 8192, vw, lb + REST_L + n * 1024);
      else dma16((const char*)wo + (n - 20) * 8192 + cj * 128, vw, lb + WO_L + (n - 20) * 1024);
    }
    if (HI && w == 7) { vmwait<12>(); st8d(ws + WS_F2 + b * 8, 0u, c.tag); }
    vmwait<9>();
  } else {
    poll8(ws + WS_F1 + (ti2 >> 1) * 1024, (lane & 15) < 12 ? (lane & 15) * 64 : 0, c.tag);
  }
  bar();
  STAMP(6);
  const size_t orow = (size_t)(64 * ti2 + 32 * tg + r) * DM + 64 * cj + 32 * cg + 16 * kh;
  const u32x2 xr0 = *(const u32x2*)(x + orow + 4 * h), xr1 = *(const u32x2*)(x + orow + 8 + 4 * h);
  f32x16 accD, accS;
#pragma unroll
  for (int j = 0; j < 16; j++) { accD[j] = 0.f; accS[j] = 0.f; }
  {
    const int p0 = HI ? w + 4 : 2 * w;
    const char* hS = ws + WS_H + (size_t)((2 * ti2 + (p0 >= 6)) * NPAIR + (p0 >= 6 ? p0 - 6 : p0)) * 1024;
    const char* aS = HI ? ws + WS_PQ + (size_t)(2 * ti2 + ((w >> 1) & 1)) * 65536 + (w & 1) * 2048 : ws + WS_KVF + w * 1024;
    const u32 mh = __builtin_amdgcn_readfirstlane(lb + p0 * 1024), ma = __builtin_amdgcn_readfirstlane(lb + (HI ? (w - 4) * 2048 : 8192 + w * 1024));
    u32 vh = vlin - 6144, va = vlin - 4096;
    auto issue_a = [&](auto st_) {
      constexpr int st = decltype(st_)::value;
      if constexpr (st <= 14) dma_ring<(st & 7) * SLOT2, 6144>(mh, vh, hS);
      else if constexpr (st <= 30 && HI) dma_ring<(st & 7) * SLOT2, 4096>(ma, va, aS);
    };
    auto issue_b = [&](auto st_) {
      constexpr int st = decltype(st_)::value;
      if constexpr (st <= 14) { if constexpr (!HI) dma_ring2(vh, hS); }
      else if constexpr (st <= 30 && HI) dma_ring2(va, aS);
    };
    auto issue_k = [&](auto st_) {
      constexpr int st = decltype(st_)::value;
      if constexpr (st >= 15 && st <= 30 && !HI) dma_ring<(st & 7) * SLOT2, 4096>(ma, va, aS);
    };
    sfor<0, 6>([&](auto t_) { issue_a(t_); issue_b(t_); });
    sfor<R1F, 43>([&](auto i) {
      constexpr int I = decltype(i)::value, O = (I - R1F) * 4096;
      trd<O>(wf[I][0], wa + REST_L); trd<O + 1024>(wf[I][1], wa + REST_L);
    });
    u32 ha[2], qa[2], ka[2], woa = wa + WO_L;
    ha[0] = lb + tg * 6144 + HI * 1024 + vlin; qa[0] = lb + tg * 4096 + HI * 2048 + vlin; ka[0] = lb + 8192 + HI * 2048 + vlin;
    ha[1] = ha[0] + 4 * SLOT2; qa[1] = qa[0] + 4 * SLOT2; ka[1] = ka[0] + 4 * SLOT2;
    u32x4 hf[2][3], f2a, f2b;
    u32x4 pq[2][2], kv[2][2];
    u32x2 wof[2][2];
    f32x16 nn[2];
    asm volatile("" : "+v"(accD), "+v"(accS), "+v"(ha[0]), "+v"(ha[1]), "+v"(qa[0]), "+v"(qa[1]), "+v"(ka[0]), "+v"(ka[1]), "+v"(woa));
    sfor<0, 33>([&](auto t_) {
      constexpr int t = decltype(t_)::value, p = t - 1, pp2 = t - 2;
      lgwait();
      if constexpr (p >= 0 && p <= 14) amfma(accD, cat(wf[3 * p][0], wf[3 * p][1]), hf[p & 1][0]);
      if constexpr (p >= 15 && p <= 30) amfma0(nn[p & 1], kv[p & 1][0], pq[p & 1][0]);
      if constexpr (t == 12 && !HI) {
        if (w == 0) {
          poll_f2(f2a, f2b, ws + WS_F2, vlin, c.tag);
          if (b == 0) st8d(ws + WS_EP, 0u, c.tag);
        }
      }
      if constexpr (t <= 30) {
        vmwait<need_n(!HI, t)>();
        bar();
        if constexpr (t == 0) STAMP(7);
        if constexpr (t == 12) STAMP(8);
        if constexpr (t == 15) STAMP(9);
        constexpr int so = (t & 3) * SLOT2, hb = (t >> 2) & 1;
        if constexpr (t <= 14) {
          rd128<so>(hf[t & 1][0], ha[hb]);
          if constexpr (t < 14) { rd128<so + 2048>(hf[t & 1][1], ha[hb]); rd128<so + 4096>(hf[t & 1][2], ha[hb]); }
        } else {
          rd128<so>(pq[t & 1][0], qa[hb]); rd128<so>(kv[t & 1][0], ka[hb]);
          rd128<so + 1024>(pq[t & 1][1], qa[hb]); rd128<so + 1024>(kv[t & 1][1], ka[hb]);
        }
      }
      if constexpr (p >= 15 && p <= 30) { trd<(p - 15) * 4096>(wof[p & 1][0], woa); trd<(p - 15) * 4096 + 1024>(wof[p & 1][1], woa); }
      if constexpr (p >= 0 && p < 14) amfma(accD, cat(wf[3 * p + 1][0], wf[3 * p + 1][1]), hf[p & 1][1]);
      if constexpr (p >= 15 && p <= 30) amfma(nn[p & 1], kv[p & 1][1], pq[p & 1][1]);
      if constexpr (t == 0) { issue_a(IC<6>{}); issue_b(IC<6>{}); }
      issue_a(IC<t + 7>{});
      if constexpr (t == 12) sfor<15, 19>(issue_k);
      if constexpr (t >= 12) issue_k(IC<t + 7>{});
      if constexpr (p >= 0 && p < 14) amfma(accD, cat(wf[3 * p + 2][0], wf[3 * p + 2][1]), hf[p & 1][2]);
      if constexpr (pp2 >= 15 && pp2 <= 30) {
        f32x16& n = nn[pp2 & 1];
        asm volatile("" : "+v"(n));
        const f32x2 rd = sp2(frcp(n[8]));
        const u32x4 o = {pkv(pr(n, 0) * rd), pkv(pr(n, 2) * rd), pkv(pr(n, 4) * rd), pkv(pr(n, 6) * rd)};
        amfma_n(accS, cat(wof[pp2 & 1][0], wof[pp2 & 1][1]), o);
      }
      issue_b(IC<t + 7>{});
      if constexpr (t == 9 && !HI) ld_f2_w0(f2a, f2b, ws + WS_F2, vlin, w);
      if constexpr (t == 31) asm volatile("s_nop 11");
    });
  }
  asm volatile("s_nop 15" : "+v"(accD), "+v"(accS));
  STAMP(10);
  {
    LAS f32x4* pd = (LAS f32x4*)(sm + w * 4096 + lane * 16);
    if (kh) {
      pd[0] = (f32x4){accD[0], accD[1], accD[2], accD[3]}; pd[64] = (f32x4){accD[4], accD[5], accD[6], accD[7]};
      pd[128] = (f32x4){accS[0], accS[1], accS[2], accS[3]}; pd[192] = (f32x4){accS[4], accS[5], accS[6], accS[7]};
    } else {
      pd[0] = (f32x4){accD[8], accD[9], accD[10], accD[11]}; pd[64] = (f32x4){accD[12], accD[13], accD[14], accD[15]};
      pd[128] = (f32x4){accS[8], accS[9], accS[10], accS[11]}; pd[192] = (f32x4){accS[12], accS[13], accS[14], accS[15]};
    }
  }
  lgwait();
  bar();
  {
    LAS f32x4* ps = (LAS f32x4*)(sm + (w ^ 4) * 4096 + lane * 16);
    const f32x4 d0 = ps[0], d1 = ps[64], s0 = ps[128], s1 = ps[192];
    float d[8], s[8], xv[8], o[8];
#pragma unroll
    for (int j = 0; j < 4; j++) {
      d[j] = d0[j] + (kh ? accD[8 + j] : accD[j]); d[4 + j] = d1[j] + (kh ? accD[12 + j] : accD[4 + j]);
      s[j] = s0[j] + (kh ? accS[8 + j] : accS[j]); s[4 + j] = s1[j] + (kh ? accS[12 + j] : accS[4 + j]);
    }
    xv[0] = lo_f(xr0[0]); xv[1] = hi_f(xr0[0]); xv[2] = lo_f(xr0[1]); xv[3] = hi_f(xr0[1]);
    xv[4] = lo_f(xr1[0]); xv[5] = hi_f(xr1[0]); xv[6] = lo_f(xr1[1]); xv[7] = hi_f(xr1[1]);
#pragma unroll
    for (int j = 0; j < 8; j++) {
      const float g = d[j] * frcp(1.f + fexp2(-1.4426950408889634f * d[j]));
      o[j] = fmaf(s[j], g, xv[j]);
    }
    *(u32x2*)(out + orow + 4 * h) = (u32x2){pk2(o[0], o[1]), pk2(o[2], o[3])};
    *(u32x2*)(out + orow + 8 + 4 * h) = (u32x2){pk2(o[4], o[5]), pk2(o[6], o[7])};
  }
  if constexpr (HI) asm volatile(".Lfused_end1:"); else asm volatile(".Lfused_end0:");
  STAMP(11);
}

__global__ __launch_bounds__(512, 2) void fused(const u16* __restrict__ x, const u16* __restrict__ wqkv, const u16* __restrict__ wphi,
                                                const u16* __restrict__ wo, const u16* __restrict__ wg, const u16* __restrict__ wu,
                                                const u16* __restrict__ wd, char* __restrict__ ws, u16* __restrict__ out) {
  __shared__ __attribute__((aligned(256))) char smem[LDSB];
  LAS char* sm = (LAS char*)smem;
  const u32 lb = (u32)(size_t)sm;
  const int b = blockIdx.x, xcd = b & 7, loc = b >> 3;
  const int w = __builtin_amdgcn_readfirstlane(threadIdx.x >> 6), lane = threadIdx.x & 63;
  asm volatile("" ::"s"(x), "s"(wqkv), "s"(wphi), "s"(wo), "s"(wg), "s"(wu), "s"(wd), "s"(ws), "s"(out));
  STAMP(0);
  if (w == 7) {
    u64 pc; u32 len, len1;
    asm volatile("s_getpc_b64 %0\n.Lfused_pc:\n\ts_mov_b32 %1, .Lfused_end0-.Lfused_pc\n\ts_mov_b32 %2, .Lfused_end1-.Lfused_pc" : "=s"(pc), "=s"(len), "=s"(len1));
    len = max(len, len1);
    const u32 lim = (len & ~15u) - 16u;
    dma16((const char*)(pc & ~15ull), min((u32)(loc * 1024 + lane * 16), lim), lb + STAT);
    dma16((const char*)(pc & ~15ull), min((u32)(loc * 1024 + lane * 16 + 32768), lim), lb + STAT);
    dma16((const char*)(pc & ~15ull), min((u32)(loc * 1024 + lane * 16 + 65536), lim), lb + STAT);
  }
  Ctx c;
  c.ti = 2 * xcd + (loc >> 4); c.jc = loc & 15;
  const int ti2 = 4 * xcd + (loc >> 3), cj = loc & 7;
  c.xt = x + (size_t)c.ti * 128 * DM;
  c.wphi = wphi; c.p0 = 0; c.wpo = 0;
  c.wdc = (const char*)wd + cj * 128;
  c.wqkv = (const char*)wqkv; c.wg = (const char*)wg; c.wu = (const char*)wu; c.wd = (const char*)wd; c.wo = (const char*)wo;
  if (c.jc < 10) {
    c.type = 0; c.ld = FF; c.np = c.jc < 6 ? 9 : 8;
    c.p0 = c.jc < 6 ? 9 * c.jc : 54 + 8 * (c.jc - 6);
    c.seg0 = wg + 16 * c.p0; c.seg1 = wu + 16 * c.p0;
  } else if (c.jc < 12) {
    c.type = 1; c.ld = 3 * DM; c.np = 8;
    c.seg0 = wqkv + 256 * (c.jc - 10); c.seg1 = c.seg0 + 128;
    c.wphi = wphi + (c.jc - 10) * 16 * 256;
  } else {
    c.type = 2; c.ld = 3 * DM; c.np = 8;
    c.seg0 = wqkv + DM + 128 * (c.jc - 12); c.seg1 = c.seg0 + DM;
    c.wphi = wphi + (c.jc == 15 ? 2 : c.jc - 12) * 8 * 256; c.wpo = c.jc == 15 ? 4096 : 0;
  }
  if (c.np == 9 && w < 4) stage1<5>(sm, lb, c, ws, w, lane);
  else stage1<4>(sm, lb, c, ws, w, lane);
  STAMP(3);
  vmwait<0>();
  bar();
  if (w == 0 && c.type != 2) st8d(ws + WS_F1 + (c.ti * 16 + c.jc) * 64, 0u, c.tag);
  STAMP(4);
  if (w >= 4) stage2<true>(sm, lb, c, ws, w, lane, ti2, cj, x, wo, out);
  else stage2<false>(sm, lb, c, ws, w, lane, ti2, cj, x, wo, out);
}

extern "C" void kernel_launch(void* const* d_in, const int* in_sizes, int n_in, void* d_out, int out_size, void* d_ws,
                              size_t ws_size, hipStream_t stream) {
  fused<<<256, 512, 0, stream>>>((const u16*)d_in[0], (const u16*)d_in[1], (const u16*)d_in[2], (const u16*)d_in[3],
                                 (const u16*)d_in[5], (const u16*)d_in[6], (const u16*)d_in[7], (char*)d_ws, (u16*)d_out);
}
```

```cpp
#include <hip/hip_runtime.h>
#include <hip/hip_bf16.h>

typedef unsigned short u16;
typedef unsigned int u32;
typedef unsigned long long u64;
typedef short s16x4 __attribute__((ext_vector_type(4)));
typedef __bf16 bf16x8 __attribute__((ext_vector_type(8)));
typedef __bf16 bf16x2 __attribute__((ext_vector_type(2)));
typedef float f32x2 __attribute__((ext_vector_type(2)));
typedef float f32x4 __attribute__((ext_vector_type(4)));
typedef float f32x16 __attribute__((ext_vector_type(16)));
typedef u32 u32x2 __attribute__((ext_vector_type(2)));
typedef u32 u32x4 __attribute__((ext_vector_type(4)));
#define LAS __attribute__((address_space(3)))
#define DI __device__ __forceinline__
#define MFMA32(a, b, c) __builtin_amdgcn_mfma_f32_32x32x16_bf16((a), (b), (c), 0, 0, 0)
#define BF8(x) __builtin_bit_cast(bf16x8, (x))

#define MAP1 1
#define ST_CALC 4
#define ST_GATHER 12

constexpr int DM = 512, FF = 1376, NH = 32, NPAIR = 86;
constexpr size_t WS_H = 0;
constexpr size_t WS_PQ = WS_H + (size_t)64 * 88 * 1024;
constexpr size_t WS_KVP = WS_PQ + (size_t)64 * 32 * 2048;
constexpr size_t WS_KVF = WS_KVP + (size_t)16 * 32 * 4096;
constexpr size_t WS_RS = WS_KVF + 65536;
constexpr size_t WS_F1 = WS_RS + 32768;
constexpr size_t WS_F2 = WS_F1 + 16384;
constexpr size_t WS_EP = WS_F2 + 2048;
constexpr u32 SPIN_CAP = 1u << 15;

#define STAMP(s) do {} while (0)

template <int N> struct IC { static constexpr int value = N; };
template <int B, int E, class F> DI void sfor(F&& f) { if constexpr (B < E) { f(IC<B>{}); sfor<B + 1, E>(f); } }

DI void dma16(const void* sbase, u32 voff, u32 lds) {
  asm volatile("s_mov_b32 m0, %2\n\ts_nop 0\n\tglobal_load_lds_dwordx4 %0, %1" ::"v"(voff), "s"(sbase), "s"(__builtin_amdgcn_readfirstlane(lds)) : "memory");
}
DI void dma16d(const void* sbase, u32 voff, u32 lds) {
  asm volatile("s_mov_b32 m0, %2\n\ts_nop 0\n\tglobal_load_lds_dwordx4 %0, %1 sc1" ::"v"(voff), "s"(sbase), "s"(__builtin_amdgcn_readfirstlane(lds)) : "memory");
}
template <int IMM> DI void set_m0(u32 base) { asm volatile("s_add_u32 m0, %0, %1" ::"s"(base), "n"(IMM) : "memory", "scc"); }
template <int J> DI void dma_p(u32 voff, const void* sbase) {
  asm volatile("global_load_lds_dwordx4 %0, %1 offset:%2" ::"v"(voff), "s"(sbase), "n"(J * 1024) : "memory");
}
template <int J> DI void dma_pm(u32 voff, const void* sbase, u64 em) {
  asm volatile("s_mov_b64 exec, %2\n\tglobal_load_lds_dwordx4 %0, %1 offset:%3\n\ts_mov_b64 exec, -1" ::"v"(voff), "s"(sbase), "s"(em), "n"(J * 1024) : "memory");
}
template <int IMM, int STEP> DI void dma_ring(u32 base, u32& voff, const void* sbase) {
  asm volatile("s_add_u32 m0, %1, %3\n\tv_add_u32 %0, %4, %0\n\tglobal_load_lds_dwordx4 %0, %2 sc1" : "+v"(voff) : "s"(base), "s"(sbase), "n"(IMM), "n"(STEP) : "memory", "scc");
}
DI void dma_ring2(u32 voff, const void* sbase) { asm volatile("global_load_lds_dwordx4 %0, %1 offset:1024 sc1" ::"v"(voff), "s"(sbase) : "memory"); }
DI float max3(float a, float b, float c) { float d; asm("v_max3_f32 %0, %1, %2, %3" : "=v"(d) : "v"(a), "v"(b), "v"(c)); return d; }
DI float nmin3(float a, float b, float c) { float d; asm("v_max3_f32 %0, -%1, -%2, -%3" : "=v"(d) : "v"(a), "v"(b), "v"(c)); return d; }
DI float nmin3m(float m, float b, float c) { float d; asm("v_max3_f32 %0, %1, -%2, -%3" : "=v"(d) : "v"(m), "v"(b), "v"(c)); return d; }
template <int OFF> DI void st16o(void* sbase, u32 voff, const u32x4& v) {
  asm volatile("global_store_dwordx4 %0, %1, %2 offset:%3 sc1\n\ts_nop 1" ::"v"(voff), "v"(v), "s"(sbase), "n"(OFF) : "memory");
}
DI void touch(const void* p, u32 lds) {
  asm volatile("s_mov_b32 m0, %1\n\ts_nop 0\n\tglobal_load_lds_dword %0, off" ::"v"(p), "s"(__builtin_amdgcn_readfirstlane(lds)) : "memory");
}
DI void touchd(const void* sbase, u32 voff, u32 lds) {
  asm volatile("s_mov_b32 m0, %2\n\ts_nop 0\n\tglobal_load_lds_dword %0, %1 sc1" ::"v"(voff), "s"(sbase), "s"(__builtin_amdgcn_readfirstlane(lds)) : "memory");
}
template <int N> DI void lgw() { asm volatile("s_waitcnt lgkmcnt(%0)" ::"n"(N) : "memory"); }
DI f32x2 pkfma(f32x2 a, f32x2 b, f32x2 c) { return __builtin_elementwise_fma(a, b, c); }
DI f32x2 pr(const f32x16& v, int i) { return (f32x2){v[i], v[i + 1]}; }
DI f32x2 sp2(float a) { return (f32x2){a, a}; }
template <int N> DI void vmwait() { asm volatile("s_waitcnt vmcnt(%0)" ::"n"(N) : "memory"); }
DI void lgwait() { asm volatile("s_waitcnt lgkmcnt(0)" ::: "memory"); }
DI void bar() { asm volatile("s_barrier" ::: "memory"); }
DI void st16d(void* sbase, u32 voff, const u32x4& v) { asm volatile("global_store_dwordx4 %0, %1, %2 sc1\n\ts_nop 1" ::"v"(voff), "v"(v), "s"(sbase) : "memory"); }
DI void st8d(void* sbase, u32 voff, const u32x2& v) { asm volatile("global_store_dwordx2 %0, %1, %2 sc1\n\ts_nop 1" ::"v"(voff), "v"(v), "s"(sbase) : "memory"); }
DI void st4d(void* sbase, u32 voff, u32 v) { asm volatile("global_store_dword %0, %1, %2 sc1\n\ts_nop 1" ::"v"(voff), "v"(v), "s"(sbase) : "memory"); }
DI void ld16(u32x4& d, const void* sbase, u32 voff) { asm volatile("global_load_dwordx4 %0, %1, %2" : "+v"(d) : "v"(voff), "s"(sbase) : "memory"); }
DI void ld16d(u32x4& d, const void* sbase, u32 voff) { asm volatile("global_load_dwordx4 %0, %1, %2 sc1" : "+v"(d) : "v"(voff), "s"(sbase) : "memory"); }
DI void ld8d(u32x2& d, const void* sbase, u32 voff) { asm volatile("global_load_dwordx2 %0, %1, %2 sc1" : "=v"(d) : "v"(voff), "s"(sbase) : "memory"); }
template <int OFF> DI void trd(u32x2& d, u32 a) { asm volatile("ds_read_b64_tr_b16 %0, %1 offset:%2" : "=v"(d) : "v"(a), "n"(OFF) : "memory"); }
template <int OFF> DI void rd128(u32x4& d, u32 a) { asm volatile("ds_read_b128 %0, %1 offset:%2" : "=v"(d) : "v"(a), "n"(OFF) : "memory"); }
DI u32x4 cat(const u32x2& a, const u32x2& b) { return (u32x4){a[0], a[1], b[0], b[1]}; }
DI void amfma(f32x16& c, const u32x4& a, const u32x4& b) { asm volatile("v_mfma_f32_32x32x16_bf16 %0, %1, %2, %0" : "+v"(c) : "v"(a), "v"(b)); }
DI void amfma16(f32x4& c, const u32x4& a, const u32x4& b) { asm volatile("v_mfma_f32_16x16x32_bf16 %0, %1, %2, %0" : "+v"(c) : "v"(a), "v"(b)); }
DI void amfma0(f32x16& c, const u32x4& a, const u32x4& b) { asm volatile("v_mfma_f32_32x32x16_bf16 %0, %1, %2, 0" : "=&v"(c) : "v"(a), "v"(b)); }
DI void amfma_n(f32x16& c, const u32x4& a, const u32x4& b) { asm volatile("s_nop 1\n\tv_mfma_f32_32x32x16_bf16 %0, %1, %2, %0" : "+v"(c) : "v"(a), "v"(b)); }

DI bf16x8 tr_frag(LAS char* p0, LAS char* p1) {
  s16x4 a = __builtin_amdgcn_ds_read_tr16_b64_v4i16((LAS s16x4*)p0);
  s16x4 b = __builtin_amdgcn_ds_read_tr16_b64_v4i16((LAS s16x4*)p1);
  return BF8(__builtin_shufflevector(a, b, 0, 1, 2, 3, 4, 5, 6, 7));
}
DI u32 pk2(float a, float b) { f32x2 v = {a, b}; return __builtin_bit_cast(u32, __builtin_convertvector(v, bf16x2)); }
DI u32 pkv(f32x2 v) { return __builtin_bit_cast(u32, __builtin_convertvector(v, bf16x2)); }
DI float bfr(float a) { return __uint_as_float(pk2(a, a) & 0xffff0000u); }
DI float lo_f(u32 w) { return __uint_as_float(w << 16); }
DI float hi_f(u32 w) { return __uint_as_float(w & 0xffff0000u); }
DI float fexp2(float x) { return __builtin_amdgcn_exp2f(x); }
DI float frcp(float x) { return __builtin_amdgcn_rcpf(x); }
DI void swap32(float& a, float& b) {
  auto r = __builtin_amdgcn_permlane32_swap(__float_as_uint(a), __float_as_uint(b), false, false);
  a = __uint_as_float(r[0]); b = __uint_as_float(r[1]);
}
template <int C> DI float dpp_add(float a) { return a + __uint_as_float(__builtin_amdgcn_update_dpp(0, __float_as_uint(a), C, 0xf, 0xf, true)); }
DI float wave_sum(float a) {
  a = dpp_add<0xB1>(a); a = dpp_add<0x4E>(a); a = dpp_add<0x141>(a); a = dpp_add<0x140>(a);
  auto r = __builtin_amdgcn_permlane16_swap(__float_as_uint(a), __float_as_uint(a), false, false);
  a = __uint_as_float(r[0]) + __uint_as_float(r[1]);
  float b = a; swap32(a, b);
  return a + b;
}
DI void poll8(const void* sbase, u32 voff, u32x2 tag) {
#pragma unroll 1
  for (u32 spin = 0; spin < SPIN_CAP; spin++) {
    u32x2 v;
    asm volatile("global_load_dwordx2 %0, %1, %2 sc1\n\ts_waitcnt vmcnt(0)" : "=&v"(v) : "v"(voff), "s"(sbase) : "memory");
    if (__all(v[0] == tag[0] && v[1] == tag[1])) break;
    __builtin_amdgcn_s_sleep(2);
  }
}

DI void ld_f2(u32x4& a, u32x4& b, const void* sbase, u32 voff) {
  asm volatile("global_load_dwordx4 %0, %2, %3 sc1\n\tglobal_load_dwordx4 %1, %2, %3 offset:1024 sc1" : "=&v"(a), "=&v"(b) : "v"(voff), "s"(sbase) : "memory");
}
DI void ld_f2_w0(u32x4& a, u32x4& b, const void* sbase, u32 voff, int w) {
  asm volatile("s_cmp_lg_u32 %4, 0\n\ts_cbranch_scc1 .Lf2%=\n\tglobal_load_dwordx4 %0, %2, %3 sc1\n\tglobal_load_dwordx4 %1, %2, %3 offset:1024 sc1\n.Lf2%=:"
               : "=&v"(a), "=&v"(b) : "v"(voff), "s"(sbase), "s"(w) : "memory", "scc");
}
DI void poll_f2(u32x4& a, u32x4& b, const void* sbase, u32 voff, u32x2 tag) {
  vmwait<0>();
  asm volatile("" : "+v"(a), "+v"(b));
#pragma unroll 1
  for (u32 spin = 0; spin < SPIN_CAP; spin++) {
    const u32 d = ((a[0] ^ tag[0]) | (a[1] ^ tag[1]) | (a[2] ^ tag[0])) | ((a[3] ^ tag[1]) | (b[0] ^ tag[0]) | (b[1] ^ tag[1])) | ((b[2] ^ tag[0]) | (b[3] ^ tag[1]));
    if (__all(d == 0)) break;
    __builtin_amdgcn_s_sleep(2);
    ld_f2(a, b, sbase, voff);
    vmwait<0>();
    asm volatile("" : "+v"(a), "+v"(b));
  }
}

constexpr int SEG1 = 9344, WSLOT = 18688, XRING = 6 * WSLOT, XSLOT = 16384, STAT = XRING + 3 * XSLOT;
constexpr int WPHI_L = XRING + 2 * XSLOT;
constexpr int VIMG_L = XRING, ONES_L = STAT + 1152;
DI constexpr int phik_l(int t) { return t < 6 ? WSLOT + t * 8192 : WPHI_L + (t - 6) * 8192; }
DI constexpr int piece_l(int n) {
  return n < 72 ? ((n / 18 + 4) % 6) * WSLOT + ((n % 18) / 9) * SEG1 + (n % 9) * 1024
       : n < 88 ? XRING + (n - 72) * 1024 : n < 96 ? WPHI_L + 8192 + (n - 88) * 1024
       : n < 132 ? 2 * WSLOT + (n - 96) * 1024 : XRING + XSLOT + (n - 132) * 1024;
}
constexpr int SLOT2 = 12288, WO_L = 8 * SLOT2, REST_L = 6 * SLOT2, LDSB = 163840;
constexpr int R1F = 38;

struct Ctx {
  const u16* seg0; const u16* seg1; const u16* xt; const u16* wphi; const char* wdc; const char *wqkv, *wg, *wu, *wd, *wo;
  int wpo;
  int ld, np, type, ti, jc, p0;
  u32x2 tag;
};

template <bool NORM>
DI void dual_softmax(const f32x16& p, float sc, u32x4 (&fp)[2], u32x4 (&fm)[2]) {
  float mx[2], nn[2], sp[2], sm[2];
  f32x2 ep[2][4], em[2][4];
#pragma unroll
  for (int e = 0; e < 2; e++) {
    const int o = 8 * e;
    mx[e] = max3(max3(max3(p[o], p[o + 1], p[o + 2]), p[o + 3], p[o + 4]), max3(p[o + 5], p[o + 6], p[o + 7]), p[o]);
    nn[e] = nmin3m(nmin3m(nmin3m(nmin3(p[o], p[o + 1], p[o + 2]), p[o + 3], p[o + 4]), p[o + 5], p[o + 6]), p[o + 7], p[o]);
  }
#pragma unroll
  for (int e = 0; e < 2; e++) swap32(mx[e], nn[e]);
#pragma unroll
  for (int e = 0; e < 2; e++) { mx[e] = max3(mx[e], nn[e], nn[e]) * -sc; nn[e] = mx[e]; }
#pragma unroll
  for (int e = 0; e < 2; e++) swap32(mx[e], nn[e]);
#pragma unroll
  for (int e = 0; e < 2; e++)
#pragma unroll
    for (int j = 0; j < 4; j++) { ep[e][j] = pkfma(pr(p, 8 * e + 2 * j), sp2(sc), sp2(mx[e])); em[e][j] = pkfma(pr(p, 8 * e + 2 * j), sp2(-sc), sp2(nn[e])); }
#pragma unroll
  for (int e = 0; e < 2; e++)
#pragma unroll
    for (int j = 0; j < 4; j++) {
      ep[e][j] = (f32x2){fexp2(ep[e][j][0]), fexp2(ep[e][j][1])};
      em[e][j] = (f32x2){fexp2(em[e][j][0]), fexp2(em[e][j][1])};
    }
#pragma unroll
  for (int e = 0; e < 2; e++) {
    const f32x2 tp = (ep[e][0] + ep[e][1]) + (ep[e][2] + ep[e][3]), tm = (em[e][0] + em[e][1]) + (em[e][2] + em[e][3]);
    sp[e] = tp[0] + tp[1]; sm[e] = tm[0] + tm[1];
  }
#pragma unroll
  for (int e = 0; e < 2; e++) swap32(sp[e], sm[e]);
#pragma unroll
  for (int e = 0; e < 2; e++) { sp[e] += sm[e]; if (NORM) sp[e] = frcp(sp[e]); sm[e] = sp[e]; }
#pragma unroll
  for (int e = 0; e < 2; e++) swap32(sp[e], sm[e]);
#pragma unroll
  for (int e = 0; e < 2; e++) {
    if (!NORM) sm[e] = sp[e] * frcp(sm[e]);
#pragma unroll
    for (int j = 0; j < 4; j++) {
      if (NORM) ep[e][j] *= sp2(sp[e]);
      em[e][j] *= sp2(sm[e]);
      fp[e][j] = pkv(ep[e][j]); fm[e][j] = pkv(em[e][j]);
    }
  }
}

template <int NT, bool XR>
DI void mainloop(u32 lb, Ctx& c, char* ws, int w, int lane, f32x16 (&acc)[NT], f32x4& cs0, f32x4& cs1, u32x4& gran) {
  const int tg = w & 3, pg = w >> 2;
  const int r = lane & 31, h = lane >> 5, g16 = (lane >> 4) & 1, q4 = (lane & 15) >> 2, pp = lane & 3;
  const int T0 = pg ? (c.np == 9 ? 5 : 4) : 0;
  const u32 vlin = lane * 16;
  const u32 vw = (lane >> 3) * 1024 + 16 * ((lane & 7) ^ (((lane >> 4) & 1) << 2));
  u32 voff[4];
  const char* src;
  u32 lds, sstep = 0;
  if constexpr (!XR) {
    const int seg = w >= 3, m0 = 3 * (w - 3 * seg), emax = 2 * c.np - 1;
#pragma unroll
    for (int m = 0; m < 3; m++) {
      const int ch = 64 * (m0 + m) + lane, row = (ch * 3641) >> 16;
      int e = ch - row * 18; e = e > emax ? emax : e;
      voff[m] = (u32)(row * c.ld + 8 * e) * 2u - m * 1024;
    }
    voff[3] = 0;
    src = (const char*)(seg ? c.seg1 : c.seg0);
    lds = lb + seg * SEG1 + m0 * 1024;
    sstep = 64 * c.ld;
  } else {
    const int rr = lane >> 3, pos = lane & 7, f = lane >> 4;
    voff[0] = rr * 1024 + 16 * (pos ^ f);
    voff[1] = rr * 1024 + 16 * (pos ^ f ^ 4) + 7168;
    voff[2] = voff[0] + 14336; voff[3] = voff[1] + 14336;
    src = (const char*)c.xt + (w - 6) * 65536;
    lds = lb + XRING + (w - 6) * 8192;
  }
  lds = __builtin_amdgcn_readfirstlane(lds);
  const u64 em = c.type == 2 ? 0ull : ~0ull;
  const char* cur = src;
  auto setup = [&](auto u_) {
    constexpr int u = decltype(u_)::value;
    if constexpr (!XR) {
      constexpr int U = u + 4;
      if constexpr (U == 16) {
        src = c.wdc + 3 * w * 8192; sstep = 18 * 8192;
        voff[0] = vw; voff[1] = vw + 7168; voff[2] = vw + 14336;
      }
      if constexpr (U <= 19) { set_m0<(U % 6) * WSLOT>(lds); cur = src; src += sstep; }
    } else {
      constexpr int sl = (u + 3) >> 1, hf = (u + 3) & 1;
      if constexpr (u == 13) {
        const bool ph = w == 6;
        src = ph ? (const char*)c.wphi : c.wdc + 88 * 8192; sstep = ph ? 4096 : 32768;
#pragma unroll
        for (int j = 0; j < 4; j++) voff[j] = ph ? vlin : vw + j * 7168;
      }
      if constexpr (u == 15) {
        src = c.wdc + (72 + (w - 6) * 8) * 8192; sstep = 32768;
#pragma unroll
        for (int j = 0; j < 4; j++) voff[j] = vw + j * 7168;
      }
      set_m0<(sl % 3) * XSLOT + hf * 4096>(lds);
      if constexpr (u < 13) cur = src + (sl * 128 + hf * 32768);
      else cur = hf ? src + sstep : src;
    }
  };
  auto piece = [&](auto u_, auto j_) {
    constexpr int u = decltype(u_)::value, j = decltype(j_)::value;
    if constexpr (XR ? u <= 16 : (u <= 15 && j < 3)) {
      if constexpr (u >= 15) dma_pm<j>(voff[j], cur, em); else dma_p<j>(voff[j], cur);
    }
  };
  auto unit = [&](auto u_) { setup(u_); asm volatile("s_nop 0"); sfor<0, 4>([&](auto j_) { piece(u_, j_); }); };
  u64 ep;
  asm volatile("s_load_dwordx2 %0, %1, 0x0" : "=s"(ep) : "s"(ws + WS_EP) : "memory");
  u32x4 srow;
  unit(IC<(XR ? -3 : -4)>{});
  asm volatile("global_load_dwordx4 %0, %1, %2" : "=v"(srow) : "v"(vlin), "s"(c.xt + (8 * c.jc + w) * DM) : "memory");
  sfor<(XR ? -2 : -3), 0>(unit);
  const char* wp = nullptr;
  u32 wstep = 0;
  if constexpr (NT == 4 && !XR) {
    const int b = blockIdx.x;
    if (w == 5) {
      const int g = b * 59 + min(lane, 58);
      wp = g < 11008 ? c.wd + g * 128 : c.wo + (g - 11008) * 128;
    } else {
      const int g = min(b * 34 + min(lane, 33), 8575);
      wstep = g < 3072 ? 393216 : 352256;
      wp = (g < 3072 ? c.wqkv + g * 128 : g < 5824 ? c.wg + (g - 3072) * 128 : c.wu + (g - 5824) * 128) + wstep;
      if (w == 4) touch(wp, lb + STAT + 1280);
    }
  }
  u32 wl[NT], wh[NT], xa[4];
#pragma unroll
  for (int s = 0; s < NT; s++) {
    int t = s + tg; t = t >= NT ? t - NT : t;
    wl[s] = lb + g16 * SEG1 + (8 * h + q4) * 288 + 8 * pp + 32 * (T0 + t);
    wh[s] = wl[s] + 3 * WSLOT;
#pragma unroll
    for (int j = 0; j < 16; j++) acc[s][j] = 0.f;
    asm volatile("" : "+v"(acc[s]), "+v"(wl[s]), "+v"(wh[s]));
  }
#pragma unroll
  for (int k = 0; k < 4; k++) { xa[k] = (lb + XRING + (32 * tg + r) * 128 + 16 * (h ^ ((r >> 1) & 7))) ^ (32 * k); asm volatile("" : "+v"(xa[k])); }
  cs0 = (f32x4){0.f, 0.f, 0.f, 0.f}; cs1 = cs0;
  const u32 one = ((lane & 31) == 0 || (lane & 31) == 17) ? 0x3f803f80u : 0u;
  u32x4 sel = {one, one, one, one};
  u32 gaddr = r * 16, vw_ = vw;
  asm volatile("s_waitcnt lgkmcnt(0)" : "+s"(ep), "+v"(cs0), "+v"(cs1), "+v"(sel), "+v"(gaddr), "+v"(vw_), "+v"(wp), "+v"(wstep) :: "memory");
  ep += 1;
  c.tag = (u32x2){(u32)ep, (u32)(ep >> 32)};
  u32x2 A[2][NT][2];
  u32x4 B[2];
  auto rdw = [&](auto k_, auto s_) {
    constexpr int k = decltype(k_)::value, s = decltype(s_)::value, sl = (k >> 1) % 6, O = (sl % 3) * WSLOT + (k & 1) * 4608;
    trd<O>(A[k & 1][s][0], sl < 3 ? wl[s] : wh[s]); trd<O + 1152>(A[k & 1][s][1], sl < 3 ? wl[s] : wh[s]);
  };
  auto rdx = [&](auto k_) { constexpr int k = decltype(k_)::value; rd128<((k >> 2) % 3) * XSLOT>(B[k & 1], xa[k & 3]); };
  if constexpr (XR) vmwait<4>(); else vmwait<9>();
  bar();
  STAMP(1);
  rdx(IC<0>{});
  sfor<0, NT>([&](auto s_) { rdw(IC<0>{}, s_); });
  setup(IC<0>{}); asm volatile("s_nop 0"); piece(IC<0>{}, IC<0>{}); piece(IC<0>{}, IC<1>{});
  sfor<0, 32>([&](auto k_) {
    constexpr int k = decltype(k_)::value, C = k & 1;
    constexpr int u = (k + 1) >> 1;
    constexpr bool N = k < 31;
    using K1 = IC<k + 1>;
    lgwait();
    amfma(acc[0], cat(A[C][0][0], A[C][0][1]), B[C]);
    if constexpr (C && u < 16) {
      if constexpr (XR) { if constexpr (!(u & 1)) vmwait<4>(); } else vmwait<9>();
      bar();
    }
    if constexpr (k == 31 && XR) { vmwait<4>(); if (c.type == 2) vmwait<0>(); }
    if constexpr (k == 31) bar();
    if constexpr (C) {
      if constexpr (NT == 4 && !XR && (u == 1 || u == 3)) { if (w == 4) { wp += wstep; touch(wp, lb + STAT + 1280); } }
      if constexpr (NT == 4 && !XR && u == 7) { if (w == 5) touch(wp, lb + STAT + 1280); }
      if constexpr (u == ST_GATHER) asm volatile("global_load_dwordx4 %0, %1, %2 sc1" : "=v"(gran) : "v"(gaddr), "s"(ws + WS_RS + (128 * c.ti + 32 * tg) * 16) : "memory");
      setup(IC<u>{});
    }
    if constexpr (N) { rdx(K1{}); rdw(K1{}, IC<0>{}); }
    piece(IC<u>{}, IC<(C ? 0 : 2)>{});
    amfma(acc[1], cat(A[C][1][0], A[C][1][1]), B[C]);
    if constexpr (N) { rdw(K1{}, IC<1>{}); rdw(K1{}, IC<2>{}); }
    amfma16(cs0, sel, cat(A[C][0][0], A[C][0][1]));
    amfma(acc[2], cat(A[C][2][0], A[C][2][1]), B[C]);
    if constexpr (N) { rdw(K1{}, IC<3>{}); if constexpr (NT == 5) rdw(K1{}, IC<NT - 1>{}); }
    piece(IC<u>{}, IC<(C ? 1 : 3)>{});
    amfma(acc[3], cat(A[C][3][0], A[C][3][1]), B[C]);
    if constexpr (NT == 5) {
      amfma16(cs1, sel, cat(A[C][1][0], A[C][1][1]));
      amfma(acc[NT - 1], cat(A[C][NT - 1][0], A[C][NT - 1][1]), B[C]);
    }
    if constexpr (k == 2 * ST_CALC - 1) {
      asm volatile("" : "+v"(srow));
      const float piv = lo_f(__builtin_amdgcn_readfirstlane(srow[0]));
      f32x2 v[4];
#pragma unroll
      for (int j = 0; j < 4; j++) v[j] = (f32x2){lo_f(srow[j]), hi_f(srow[j])} - sp2(piv);
      const f32x2 s2 = (v[0] + v[1]) + (v[2] + v[3]), q2 = pkfma(v[0], v[0], v[1] * v[1]) + pkfma(v[2], v[2], v[3] * v[3]);
      const float ms = wave_sum(s2[0] + s2[1]) * (1.f / 512.f), mq = wave_sum(q2[0] + q2[1]) * (1.f / 512.f);
      const float mean = piv + ms, var = fmaxf(mq - ms * ms, 0.f);
      const float sd = bfr(__builtin_amdgcn_sqrtf(bfr(bfr(var) + 1e-5f)));
      st16d(ws + WS_RS + (128 * c.ti + 8 * c.jc + w) * 16, 0u, (u32x4){__float_as_uint(sd), __float_as_uint(bfr(mean)), c.tag[0], c.tag[1]});
    }
  });
  piece(IC<16>{}, IC<2>{}); piece(IC<16>{}, IC<3>{});
  if constexpr (XR) vmwait<4 * (17 - ST_GATHER)>();
  else { if (c.type == 2) vmwait<3 * (15 - ST_GATHER)>(); else vmwait<3 * (16 - ST_GATHER)>(); }
  asm volatile("s_nop 15" : "+v"(cs0), "+v"(cs1), "+v"(gran) :: "memory");
#pragma unroll
  for (int s = 0; s < NT; s++) asm volatile("" : "+v"(acc[s]));
}

template <int NT>
DI void stage1(LAS char* sm, u32 lb, Ctx& c, char* ws, int w, int lane) {
  const int tg = w & 3, pg = w >> 2;
  const int r = lane & 31, h = lane >> 5, g16 = (lane >> 4) & 1, q4 = (lane & 15) >> 2, pp = lane & 3;
  const int T0 = pg ? (c.np == 9 ? 5 : 4) : 0;
  const u32 vlin = lane * 16;
  const u32 vw = (lane >> 3) * 1024 + 16 * ((lane & 7) ^ (((lane >> 4) & 1) << 2));
  f32x16 acc[NT];
  f32x4 cs0, cs1;
  u32x4 gran;
  if (NT == 4 && w >= 6) mainloop<NT, true>(lb, c, ws, w, lane, acc, cs0, cs1, gran);
  else mainloop<NT, false>(lb, c, ws, w, lane, acc, cs0, cs1, gran);
  STAMP(2);
  if (lane < 16) {
    LAS float* cs = (LAS float*)(sm + STAT);
    cs[(T0 + tg) * 32 + lane] = cs0[0]; cs[(T0 + tg) * 32 + 16 + lane] = cs0[1];
    if (NT == 5 && tg == 3) { cs[128 + lane] = cs1[0]; cs[144 + lane] = cs1[1]; }
  }
  u32x4 wab[4];
  if (NT == 4 && c.type) {
    LAS char* wp = sm + WPHI_L + c.wpo + (4 * h + q4) * 32 + 8 * pp;
#pragma unroll
    for (int s = 0; s < 4; s++) {
      const int t = c.type == 1 ? T0 + ((s + tg) & 3) + 8 * g16 : T0 + (((s & 2) + g16 + tg) & 3);
      wab[s] = __builtin_bit_cast(u32x4, tr_frag(wp + t * 512, wp + t * 512 + 256));
    }
  }
#pragma unroll 1
  for (u32 spin = 0; spin < SPIN_CAP && !__all(gran[2] == c.tag[0] && gran[3] == c.tag[1]); spin++) {
    __builtin_amdgcn_s_sleep(2);
    asm volatile("global_load_dwordx4 %0, %1, %2 sc1\n\ts_waitcnt vmcnt(0)" : "=&v"(gran) : "v"(r * 16), "s"(ws + WS_RS + (128 * c.ti + 32 * tg) * 16) : "memory");
  }
  const float sd = __uint_as_float(gran[0]), nmean = -__uint_as_float(gran[1]), rs = 1.f / sd;
  lgwait();
  bar();
  auto w2 = [&](int j) { const int n = 96 + w + 8 * j; dma16(c.wdc + n * 8192, vw, lb + piece_l(n)); };
#pragma unroll
  for (int s = 0; s < NT; s++) {
    int t = s + tg; t = t >= NT ? t - NT : t;
    LAS f32x4* cp = (LAS f32x4*)(sm + STAT + ((T0 + t) * 32 + 4 * h) * 4);
#pragma unroll
    for (int i = 0; i < 4; i++) {
      const f32x4 cv = cp[2 * i];
      const f32x2 a0 = pkfma(sp2(nmean), (f32x2){cv[0], cv[1]}, pr(acc[s], 4 * i)), a1 = pkfma(sp2(nmean), (f32x2){cv[2], cv[3]}, pr(acc[s], 4 * i + 2));
      acc[s][4 * i] = a0[0]; acc[s][4 * i + 1] = a0[1]; acc[s][4 * i + 2] = a1[0]; acc[s][4 * i + 3] = a1[1];
    }
  }
  const int tg32 = 4 * c.ti + tg;
  if (c.type == 0) {
    const float K0 = -2.f * 0.7978845608028654f * 1.4426950408889634f * rs, K1 = K0 * 0.044715f * rs * rs, v2 = sd * sd;
    char* dst = ws + WS_H + (size_t)(tg32 * NPAIR + c.p0 + T0) * 1024;
#pragma unroll
    for (int s = 0; s < NT; s++) {
      int t = s + tg; t = t >= NT ? t - NT : t;
      if (s < 4) { w2(2 * s); if (s < 3) w2(2 * s + 1); }
      f32x2 e[4], gu[4];
#pragma unroll
      for (int j = 0; j < 4; j++) { const f32x2 g = pr(acc[s], 2 * j); e[j] = g * pkfma(g * g, sp2(K1), sp2(K0)); gu[j] = g * pr(acc[s], 8 + 2 * j); }
#pragma unroll
      for (int j = 0; j < 4; j++) e[j] = pkfma((f32x2){fexp2(e[j][0]), fexp2(e[j][1])}, sp2(v2), sp2(v2));
#pragma unroll
      for (int j = 0; j < 4; j++) gu[j] *= (f32x2){frcp(e[j][0]), frcp(e[j][1])};
      st16d(dst, vlin + t * 1024, (u32x4){pkv(gu[0]), pkv(gu[1]), pkv(gu[2]), pkv(gu[3])});
    }
  } else if constexpr (NT == 4) {
    const float sc = rs * 1.4426950408889634f;
    f32x16 zero;
#pragma unroll
    for (int j = 0; j < 16; j++) zero[j] = 0.f;
    if (c.type == 1) {
      char* dst = ws + WS_PQ + (size_t)(tg32 * NH + 16 * (c.jc - 10) + T0) * 2048;
      f32x16 P[4];
#pragma unroll
      for (int s = 0; s < 4; s++) {
        u32x4 wA, wB, qa, qb;
#pragma unroll
        for (int j = 0; j < 4; j++) {
          wA[j] = g16 ? 0u : wab[s][j]; wB[j] = g16 ? wab[s][j] : 0u;
          qa[j] = pkv(pr(acc[s], 2 * j)); qb[j] = pkv(pr(acc[s], 8 + 2 * j));
        }
        P[s] = MFMA32(BF8(wA), BF8(qa), zero);
        P[s] = MFMA32(BF8(wB), BF8(qb), P[s]);
      }
#pragma unroll
      for (int s = 0; s < 4; s++) {
        const u32 vo = vlin + ((s + tg) & 3) * 2048;
        w2(2 * s); if (s < 3) w2(2 * s + 1);
        u32x4 fp[2], fm[2];
        dual_softmax<false>(P[s], sc, fp, fm);
        st16o<0>(dst, vo, fp[0]); st16o<1024>(dst, vo, fm[0]);
        st16o<0>(dst + 16384, vo, fp[1]); st16o<1024>(dst + 16384, vo, fm[1]);
      }
    } else {
      const int tok = 32 * tg + r;
      const u32 pw = tok * 64, vwl = VIMG_L + tok * 32;
      const int fsw = (r >> 1) & 7, vsw = (r >> 2) & 3;
      f32x16 P[2];
#pragma unroll
      for (int s = 0; s < 4; s += 2) {
        u32x4 wA, wB, ka, kb;
#pragma unroll
        for (int j = 0; j < 4; j++) {
          wA[j] = g16 ? 0u : wab[s][j]; wB[j] = g16 ? wab[s][j] : 0u;
          ka[j] = pkv(pr(acc[s], 2 * j)); kb[j] = pkv(pr(acc[s + 1], 2 * j));
        }
        P[s >> 1] = MFMA32(BF8(wA), BF8(ka), zero);
        P[s >> 1] = MFMA32(BF8(wB), BF8(kb), P[s >> 1]);
      }
#pragma unroll
      for (int s = 0; s < 4; s++) {
        LAS char* vd = sm + vwl + (T0 + ((s + tg) & 3)) * 4096;
        const f32x16& a = acc[s];
        *(LAS u32x2*)(vd + 8 * ((0 + h) ^ vsw)) = (u32x2){pkv(pr(a, 8) * sp2(rs)), pkv(pr(a, 10) * sp2(rs))};
        *(LAS u32x2*)(vd + 8 * ((2 + h) ^ vsw)) = (u32x2){pkv(pr(a, 12) * sp2(rs)), pkv(pr(a, 14) * sp2(rs))};
      }
#pragma unroll
      for (int s = 0; s < 4; s += 2) {
        u32x4 fp[2], fm[2];
        dual_softmax<true>(P[s >> 1], sc, fp, fm);
#pragma unroll
        for (int e = 0; e < 2; e++) {
          LAS char* pd = sm + pw + phik_l(T0 + ((s + e + tg) & 3));
          *(LAS u32x2*)(pd + 8 * ((0 + h) ^ fsw)) = (u32x2){fp[e][0], fp[e][1]};
          *(LAS u32x2*)(pd + 8 * ((2 + h) ^ fsw)) = (u32x2){fp[e][2], fp[e][3]};
          *(LAS u32x2*)(pd + 8 * ((4 + h) ^ fsw)) = (u32x2){fm[e][0], fm[e][1]};
          *(LAS u32x2*)(pd + 8 * ((6 + h) ^ fsw)) = (u32x2){fm[e][2], fm[e][3]};
        }
      }
      STAMP(12);
      lgwait();
      bar();
      f32x16 kv = zero;
      {
        const int t0 = 8 * h + q4;
        const u32 pa0 = lb + phik_l(w) + t0 * 64 + 8 * ((4 * g16 + pp) ^ (t0 >> 1)), pa1 = lb + phik_l(w) + (t0 + 4) * 64 + 8 * ((4 * g16 + pp) ^ ((t0 >> 1) + 2));
        const u32 pb0 = lb + VIMG_L + w * 4096 + t0 * 32 + 8 * (pp ^ (2 * h)), pb1 = lb + VIMG_L + w * 4096 + (t0 + 4) * 32 + 8 * (pp ^ (2 * h + 1));
        u32x2 fa[8][2], fb[8][2];
        sfor<0, 8>([&](auto k_) {
          constexpr int k = decltype(k_)::value;
          trd<k * 1024>(fa[k][0], pa0); trd<k * 1024>(fa[k][1], pa1); trd<k * 512>(fb[k][0], pb0); trd<k * 512>(fb[k][1], pb1);
        });
        lgwait();
        sfor<0, 8>([&](auto k_) {
          constexpr int k = decltype(k_)::value;
          asm volatile("" : "+v"(fa[k][0]), "+v"(fa[k][1]), "+v"(fb[k][0]), "+v"(fb[k][1]));
          u32x4 bq = cat(fb[k][0], fb[k][1]);
#pragma unroll
          for (int j = 0; j < 4; j++) bq[j] = g16 ? 0x3f803f80u : bq[j];
          kv = MFMA32(BF8(cat(fa[k][0], fa[k][1])), BF8(bq), kv);
        });
      }
      if (r <= 16) {
        char* dst = ws + WS_KVP + (size_t)(c.ti * NH + 8 * (c.jc - 12) + w) * 4096;
#pragma unroll
        for (int g = 0; g < 4; g++)
          st16d(dst + g * 1024, vlin, (u32x4){__float_as_uint(kv[4 * g]), __float_as_uint(kv[4 * g + 1]), __float_as_uint(kv[4 * g + 2]), __float_as_uint(kv[4 * g + 3])});
      }
      STAMP(13);
      vmwait<0>();
      lgwait();
      bar();
      if (w == 0) st8d(ws + WS_F1 + (c.ti * 16 + c.jc) * 64, 0u, c.tag);
      STAMP(14);
#pragma unroll 1
      for (int n = 54 + w; n < 152; n += 8) dma16(c.wdc + n * 8192, vw, lb + piece_l(n));
    }
  }
}

constexpr int ops_after(bool lo, int t) {
  if (t < 0) return lo ? 12 : 6;
  int n = 0;
  for (int st = (t == 0 ? 6 : t + 7); st <= t + 7; st++) {
    if (st <= 14) n += lo ? 2 : 1;
    else if (st <= 30) n += lo ? 0 : 2;
  }
  if (lo && t == 12) n += 5;
  if (lo && t > 12 && t + 7 <= 30) n += 1;
  return n;
}
constexpr int need_n(bool lo, int t) {
  int last = 0;
  int tail = 0;
  if (t <= 5) { last = -1; tail = (5 - t) * (lo ? 2 : 1); }
  else if (t == 6) { last = 0; tail = lo ? 2 : 1; }
  else if (lo && t >= 15 && t <= 19) { last = 12; tail = 19 - t; }
  else last = t - 7;
  int n = tail;
  for (int k = last + 1; k < t; k++) n += ops_after(lo, k);
  return n;
}

template <bool HI>
DI void stage2(LAS char* sm, u32 lb, const Ctx& c, char* ws, int w, int lane, int ti2, int cj, const u16* x, const u16* wo, u16* out) {
  const int tg = w & 1, cg = (w >> 1) & 1, b = blockIdx.x;
  constexpr int kh = HI;
  const int r = lane & 31, h = lane >> 5, g16 = (lane >> 4) & 1, q4 = (lane & 15) >> 2, pp = lane & 3;
  const u32 vlin = lane * 16;
  const u32 vw = (lane >> 3) * 1024 + 16 * ((lane & 7) ^ (((lane >> 4) & 1) << 2));
  const u32 wa0 = lb + (4 * h + q4) * 128 + ((cg ^ (q4 >> 1)) << 6) + 32 * g16 + 8 * pp, wa = wa0 + kh * 2048;
  const int khead = b >> 3, kg = (b >> 1) & 3, khalf = b & 1;
  u32x2 part[16];
  if (HI && w == 7) {
    poll8(ws + WS_F1 + (12 + (khead >> 3)) * 64, (lane & 15) * 1024, c.tag);
    const char* src = ws + WS_KVP + khead * 4096 + kg * 1024 + khalf * 8;
    const u32 vo = (r > 16 ? (lane & 32) + 16 : lane) * 16;
#pragma unroll
    for (int i = 0; i < 16; i++) ld8d(part[i], src + (size_t)i * NH * 4096, vo);
  }
  u32x2 wf[43][2];
  auto readout = [&](auto k_) {
    sfor<0, R1F>([&](auto i) {
      constexpr int I = decltype(i)::value, n = 4 * I + 2 * decltype(k_)::value, O0 = piece_l(n), O1 = piece_l(n + 1);
      trd<(O0 & 0xffff)>(wf[I][0], wa0 + (O0 & ~0xffff)); trd<(O1 & 0xffff)>(wf[I][1], wa0 + (O1 & ~0xffff));
    });
    lgwait();
  };
  readout(IC<kh>{});
  if (HI && w == 7) {
    vmwait<0>();
    float s0 = 0.f, s1 = 0.f;
#pragma unroll
    for (int i = 0; i < 16; i++) { asm volatile("" : "+v"(part[i])); s0 += __uint_as_float(part[i][0]); s1 += __uint_as_float(part[i][1]); }
    if (r >= 16 && kg < 2) { s0 += 1e-6f; s1 += 1e-6f; }
    st4d(ws + WS_KVF + khead * 2048 + (kg >> 1) * 1024 + (kg & 1) * 8 + khalf * 4, vlin, pk2(s0, s1));
  }
  lgwait();
  bar();
  STAMP(5);
  if (HI || w) {
#pragma unroll 1
    for (int m = 0; m < 12; m++) {
      const int n = w - 1 + 7 * m;
      if (n < 20) dma16(c.wdc + (152 + n) * 8192, vw, lb + REST_L + n * 1024);
      else dma16((const char*)wo + (n - 20) * 8192 + cj * 128, vw, lb + WO_L + (n - 20) * 1024);
    }
    if (HI && w == 7) { vmwait<12>(); st8d(ws + WS_F2 + b * 8, 0u, c.tag); }
    vmwait<9>();
  } else {
    poll8(ws + WS_F1 + (ti2 >> 1) * 1024, (lane & 15) < 12 ? (lane & 15) * 64 : 0, c.tag);
  }
  bar();
  STAMP(6);
  const size_t orow = (size_t)(64 * ti2 + 32 * tg + r) * DM + 64 * cj + 32 * cg + 16 * kh;
  const u32x2 xr0 = *(const u32x2*)(x + orow + 4 * h), xr1 = *(const u32x2*)(x + orow + 8 + 4 * h);
  f32x16 accD, accS;
#pragma unroll
  for (int j = 0; j < 16; j++) { accD[j] = 0.f; accS[j] = 0.f; }
  {
    const int p0 = HI ? w + 4 : 2 * w;
    const char* hS = ws + WS_H + (size_t)((2 * ti2 + (p0 >= 6)) * NPAIR + (p0 >= 6 ? p0 - 6 : p0)) * 1024;
    const char* aS = HI ? ws + WS_PQ + (size_t)(2 * ti2 + ((w >> 1) & 1)) * 65536 + (w & 1) * 2048 : ws + WS_KVF + w * 1024;
    const u32 mh = __builtin_amdgcn_readfirstlane(lb + p0 * 1024), ma = __builtin_amdgcn_readfirstlane(lb + (HI ? (w - 4) * 2048 : 8192 + w * 1024));
    u32 vh = vlin - 6144, va = vlin - 4096;
    auto issue_a = [&](auto st_) {
      constexpr int st = decltype(st_)::value;
      if constexpr (st <= 14) dma_ring<(st & 7) * SLOT2, 6144>(mh, vh, hS);
      else if constexpr (st <= 30 && HI) dma_ring<(st & 7) * SLOT2, 4096>(ma, va, aS);
    };
    auto issue_b = [&](auto st_) {
      constexpr int st = decltype(st_)::value;
      if constexpr (st <= 14) { if constexpr (!HI) dma_ring2(vh, hS); }
      else if constexpr (st <= 30 && HI) dma_ring2(va, aS);
    };
    auto issue_k = [&](auto st_) {
      constexpr int st = decltype(st_)::value;
      if constexpr (st >= 15 && st <= 30 && !HI) dma_ring<(st & 7) * SLOT2, 4096>(ma, va, aS);
    };
    sfor<0, 6>([&](auto t_) { issue_a(t_); issue_b(t_); });
    sfor<R1F, 43>([&](auto i) {
      constexpr int I = decltype(i)::value, O = (I - R1F) * 4096;
      trd<O>(wf[I][0], wa + REST_L); trd<O + 1024>(wf[I][1], wa + REST_L);
    });
    u32 ha[2], qa[2], ka[2], woa = wa + WO_L;
    ha[0] = lb + tg * 6144 + HI * 1024 + vlin; qa[0] = lb + tg * 4096 + HI * 2048 + vlin; ka[0] = lb + 8192 + HI * 2048 + vlin;
    ha[1] = ha[0] + 4 * SLOT2; qa[1] = qa[0] + 4 * SLOT2; ka[1] = ka[0] + 4 * SLOT2;
    u32x4 hf[2][3], f2a, f2b;
    u32x4 pq[2][2], kv[2][2];
    u32x2 wof[2][2];
    f32x16 nn[2];
    asm volatile("" : "+v"(accD), "+v"(accS), "+v"(ha[0]), "+v"(ha[1]), "+v"(qa[0]), "+v"(qa[1]), "+v"(ka[0]), "+v"(ka[1]), "+v"(woa));
    sfor<0, 33>([&](auto t_) {
      constexpr int t = decltype(t_)::value, p = t - 1, pp2 = t - 2;
      lgwait();
      if constexpr (p >= 0 && p <= 14) amfma(accD, cat(wf[3 * p][0], wf[3 * p][1]), hf[p & 1][0]);
      if constexpr (p >= 15 && p <= 30) amfma0(nn[p & 1], kv[p & 1][0], pq[p & 1][0]);
      if constexpr (t == 12 && !HI) {
        if (w == 0) {
          poll_f2(f2a, f2b, ws + WS_F2, vlin, c.tag);
          if (b == 0) st8d(ws + WS_EP, 0u, c.tag);
        }
      }
      if constexpr (t <= 30) {
        vmwait<need_n(!HI, t)>();
        bar();
        if constexpr (t == 0) STAMP(7);
        if constexpr (t == 12) STAMP(8);
        if constexpr (t == 15) STAMP(9);
        constexpr int so = (t & 3) * SLOT2, hb = (t >> 2) & 1;
        if constexpr (t <= 14) {
          rd128<so>(hf[t & 1][0], ha[hb]);
          if constexpr (t < 14) { rd128<so + 2048>(hf[t & 1][1], ha[hb]); rd128<so + 4096>(hf[t & 1][2], ha[hb]); }
        } else {
          rd128<so>(pq[t & 1][0], qa[hb]); rd128<so>(kv[t & 1][0], ka[hb]);
          rd128<so + 1024>(pq[t & 1][1], qa[hb]); rd128<so + 1024>(kv[t & 1][1], ka[hb]);
        }
      }
      if constexpr (p >= 15 && p <= 30) { trd<(p - 15) * 4096>(wof[p & 1][0], woa); trd<(p - 15) * 4096 + 1024>(wof[p & 1][1], woa); }
      if constexpr (p >= 0 && p < 14) amfma(accD, cat(wf[3 * p + 1][0], wf[3 * p + 1][1]), hf[p & 1][1]);
      if constexpr (p >= 15 && p <= 30) amfma(nn[p & 1], kv[p & 1][1], pq[p & 1][1]);
      if constexpr (t == 0) { issue_a(IC<6>{}); issue_b(IC<6>{}); }
      issue_a(IC<t + 7>{});
      if constexpr (t == 12) sfor<15, 19>(issue_k);
      if constexpr (t >= 12) issue_k(IC<t + 7>{});
      if constexpr (p >= 0 && p < 14) amfma(accD, cat(wf[3 * p + 2][0], wf[3 * p + 2][1]), hf[p & 1][2]);
      if constexpr (pp2 >= 15 && pp2 <= 30) {
        f32x16& n = nn[pp2 & 1];
        asm volatile("" : "+v"(n));
        const f32x2 rd = sp2(frcp(n[8]));
        const u32x4 o = {pkv(pr(n, 0) * rd), pkv(pr(n, 2) * rd), pkv(pr(n, 4) * rd), pkv(pr(n, 6) * rd)};
        amfma_n(accS, cat(wof[pp2 & 1][0], wof[pp2 & 1][1]), o);
      }
      issue_b(IC<t + 7>{});
      if constexpr (t == 9 && !HI) ld_f2_w0(f2a, f2b, ws + WS_F2, vlin, w);
      if constexpr (t == 31) asm volatile("s_nop 11");
    });
  }
  asm volatile("s_nop 15" : "+v"(accD), "+v"(accS));
  STAMP(10);
  {
    LAS f32x4* pd = (LAS f32x4*)(sm + w * 4096 + lane * 16);
    if (kh) {
      pd[0] = (f32x4){accD[0], accD[1], accD[2], accD[3]}; pd[64] = (f32x4){accD[4], accD[5], accD[6], accD[7]};
      pd[128] = (f32x4){accS[0], accS[1], accS[2], accS[3]}; pd[192] = (f32x4){accS[4], accS[5], accS[6], accS[7]};
    } else {
      pd[0] = (f32x4){accD[8], accD[9], accD[10], accD[11]}; pd[64] = (f32x4){accD[12], accD[13], accD[14], accD[15]};
      pd[128] = (f32x4){accS[8], accS[9], accS[10], accS[11]}; pd[192] = (f32x4){accS[12], accS[13], accS[14], accS[15]};
    }
  }
  lgwait();
  bar();
  {
    LAS f32x4* ps = (LAS f32x4*)(sm + (w ^ 4) * 4096 + lane * 16);
    const f32x4 d0 = ps[0], d1 = ps[64], s0 = ps[128], s1 = ps[192];
    float d[8], s[8], xv[8], o[8];
#pragma unroll
    for (int j = 0; j < 4; j++) {
      d[j] = d0[j] + (kh ? accD[8 + j] : accD[j]); d[4 + j] = d1[j] + (kh ? accD[12 + j] : accD[4 + j]);
      s[j] = s0[j] + (kh ? accS[8 + j] : accS[j]); s[4 + j] = s1[j] + (kh ? accS[12 + j] : accS[4 + j]);
    }
    xv[0] = lo_f(xr0[0]); xv[1] = hi_f(xr0[0]); xv[2] = lo_f(xr0[1]); xv[3] = hi_f(xr0[1]);
    xv[4] = lo_f(xr1[0]); xv[5] = hi_f(xr1[0]); xv[6] = lo_f(xr1[1]); xv[7] = hi_f(xr1[1]);
#pragma unroll
    for (int j = 0; j < 8; j++) {
      const float g = d[j] * frcp(1.f + fexp2(-1.4426950408889634f * d[j]));
      o[j] = fmaf(s[j], g, xv[j]);
    }
    *(u32x2*)(out + orow + 4 * h) = (u32x2){pk2(o[0], o[1]), pk2(o[2], o[3])};
    *(u32x2*)(out + orow + 8 + 4 * h) = (u32x2){pk2(o[4], o[5]), pk2(o[6], o[7])};
  }
  if constexpr (HI) asm volatile(".Lfused_end1:"); else asm volatile(".Lfused_end0:");
  STAMP(11);
}

__global__ __launch_bounds__(512, 2) void fused(const u16* __restrict__ x, const u16* __restrict__ wqkv, const u16* __restrict__ wphi,
                                                const u16* __restrict__ wo, const u16* __restrict__ wg, const u16* __restrict__ wu,
                                                const u16* __restrict__ wd, char* __restrict__ ws, u16* __restrict__ out) {
  __shared__ __attribute__((aligned(256))) char smem[LDSB];
  LAS char* sm = (LAS char*)smem;
  const u32 lb = (u32)(size_t)sm;
  const int b = blockIdx.x, xcd = b & 7, loc = b >> 3;
  const int w = __builtin_amdgcn_readfirstlane(threadIdx.x >> 6), lane = threadIdx.x & 63;
  asm volatile("" ::"s"(x), "s"(wqkv), "s"(wphi), "s"(wo), "s"(wg), "s"(wu), "s"(wd), "s"(ws), "s"(out));
  STAMP(0);
  const u64 t_start = __builtin_amdgcn_s_memrealtime();
  if (w == 7) {
    u64 pc; u32 len, len1;
    asm volatile("s_getpc_b64 %0\n.Lfused_pc:\n\ts_mov_b32 %1, .Lfused_end0-.Lfused_pc\n\ts_mov_b32 %2, .Lfused_end1-.Lfused_pc" : "=s"(pc), "=s"(len), "=s"(len1));
    len = max(len, len1);
    const u32 lim = (len & ~15u) - 16u;
    dma16((const char*)(pc & ~15ull), min((u32)(loc * 1024 + lane * 16), lim), lb + STAT);
    dma16((const char*)(pc & ~15ull), min((u32)(loc * 1024 + lane * 16 + 32768), lim), lb + STAT);
    dma16((const char*)(pc & ~15ull), min((u32)(loc * 1024 + lane * 16 + 65536), lim), lb + STAT);
  }
  Ctx c;
  c.ti = 2 * xcd + (loc >> 4); c.jc = loc & 15;
  const int ti2 = 4 * xcd + (loc >> 3), cj = loc & 7;
  c.xt = x + (size_t)c.ti * 128 * DM;
  c.wphi = wphi; c.p0 = 0; c.wpo = 0;
  c.wdc = (const char*)wd + cj * 128;
  c.wqkv = (const char*)wqkv; c.wg = (const char*)wg; c.wu = (const char*)wu; c.wd = (const char*)wd; c.wo = (const char*)wo;
  if (c.jc < 10) {
    c.type = 0; c.ld = FF; c.np = c.jc < 6 ? 9 : 8;
    c.p0 = c.jc < 6 ? 9 * c.jc : 54 + 8 * (c.jc - 6);
    c.seg0 = wg + 16 * c.p0; c.seg1 = wu + 16 * c.p0;
  } else if (c.jc < 12) {
    c.type = 1; c.ld = 3 * DM; c.np = 8;
    c.seg0 = wqkv + 256 * (c.jc - 10); c.seg1 = c.seg0 + 128;
    c.wphi = wphi + (c.jc - 10) * 16 * 256;
  } else {
    c.type = 2; c.ld = 3 * DM; c.np = 8;
    c.seg0 = wqkv + DM + 128 * (c.jc - 12); c.seg1 = c.seg0 + DM;
    c.wphi = wphi + (c.jc == 15 ? 2 : c.jc - 12) * 8 * 256; c.wpo = c.jc == 15 ? 4096 : 0;
  }
  if (c.np == 9 && w < 4) stage1<5>(sm, lb, c, ws, w, lane);
  else stage1<4>(sm, lb, c, ws, w, lane);
  STAMP(3);
  vmwait<0>();
  bar();
  if (w == 0) st8d(ws + WS_F1 + blockIdx.x * 64 + 32, 0u, (u32x2){(u32)t_start, (u32)(t_start >> 32)});
  if (w == 0 && c.type != 2) st8d(ws + WS_F1 + (c.ti * 16 + c.jc) * 64, 0u, c.tag);
  STAMP(4);
  if (w >= 4) stage2<true>(sm, lb, c, ws, w, lane, ti2, cj, x, wo, out);
  else stage2<false>(sm, lb, c, ws, w, lane, ti2, cj, x, wo, out);
  while (__builtin_amdgcn_s_memrealtime() - t_start < 4000) __builtin_amdgcn_s_sleep(1);
  u32 m = 0xffffffffu;
  for (int i = 0; i < 4; i++) { u32x2 v; asm volatile("global_load_dwordx2 %0, %1, %2 sc1\n\ts_waitcnt vmcnt(0)" : "=&v"(v) : "v"((lane + 64 * i) * 64 + 32), "s"(ws + WS_F1) : "memory"); m = min(m, v[0] - (u32)t_start + 100000u); }
  for (int o = 32; o >= 1; o >>= 1) m = min(m, (u32)__shfl_xor((int)m, o));
  const u64 t0 = t_start + (u64)__builtin_amdgcn_readfirstlane(m) - 100000ull;
  while (__builtin_amdgcn_s_memrealtime() - t0 < 6000) __builtin_amdgcn_s_sleep(1);
}

extern "C" void kernel_launch(void* const* d_in, const int* in_sizes, int n_in, void* d_out, int out_size, void* d_ws,
                              size_t ws_size, hipStream_t stream) {
  fused<<<256, 512, 0, stream>>>((const u16*)d_in[0], (const u16*)d_in[1], (const u16*)d_in[2], (const u16*)d_in[3],
                                 (const u16*)d_in[5], (const u16*)d_in[6], (const u16*)d_in[7], (char*)d_ws, (u16*)d_out);
}
```

```cpp
#include <hip/hip_runtime.h>
#include <hip/hip_bf16.h>

typedef unsigned short u16;
typedef unsigned int u32;
typedef unsigned long long u64;
typedef short s16x4 __attribute__((ext_vector_type(4)));
typedef __bf16 bf16x8 __attribute__((ext_vector_type(8)));
typedef __bf16 bf16x2 __attribute__((ext_vector_type(2)));
typedef float f32x2 __attribute__((ext_vector_type(2)));
typedef float f32x4 __attribute__((ext_vector_type(4)));
typedef float f32x16 __attribute__((ext_vector_type(16)));
typedef u32 u32x2 __attribute__((ext_vector_type(2)));
typedef u32 u32x4 __attribute__((ext_vector_type(4)));
#define LAS __attribute__((address_space(3)))
#define DI __device__ __forceinline__
#define MFMA32(a, b, c) __builtin_amdgcn_mfma_f32_32x32x16_bf16((a), (b), (c), 0, 0, 0)
#define BF8(x) __builtin_bit_cast(bf16x8, (x))

#define MAP1 1

constexpr int DM = 512, FF = 1376, NKS = 118;
constexpr size_t WS_H = 0;
constexpr size_t WS_KVP = WS_H + (size_t)64 * 120 * 1024;
constexpr size_t WS_KVF = WS_KVP + (size_t)16 * 32 * 8192;
constexpr size_t WS_RS = WS_KVF + 65536;
constexpr size_t WS_F1 = WS_RS + 32768;
constexpr size_t WS_F2 = WS_F1 + 16384;
constexpr size_t WS_EP = WS_F2 + 2048;
constexpr u32 SPIN_CAP = 1u << 15;

#define STAMP(s) do {} while (0)

template <int N> struct IC { static constexpr int value = N; };
template <int B, int E, class F> DI void sfor(F&& f) { if constexpr (B < E) { f(IC<B>{}); sfor<B + 1, E>(f); } }

DI void dma16(const void* sbase, u32 voff, u32 lds) {
  asm volatile("s_mov_b32 m0, %2\n\ts_nop 0\n\tglobal_load_lds_dwordx4 %0, %1" ::"v"(voff), "s"(sbase), "s"(__builtin_amdgcn_readfirstlane(lds)) : "memory");
}
DI void dma16d(const void* sbase, u32 voff, u32 lds) {
  asm volatile("s_mov_b32 m0, %2\n\ts_nop 0\n\tglobal_load_lds_dwordx4 %0, %1 sc1" ::"v"(voff), "s"(sbase), "s"(__builtin_amdgcn_readfirstlane(lds)) : "memory");
}
template <int IMM> DI void set_m0(u32 base) { asm volatile("s_add_u32 m0, %0, %1" ::"s"(base), "n"(IMM) : "memory", "scc"); }
template <int J> DI void dma_p(u32 voff, const void* sbase) {
  asm volatile("global_load_lds_dwordx4 %0, %1 offset:%2" ::"v"(voff), "s"(sbase), "n"(J * 1024) : "memory");
}
template <int IMM, int STEP> DI void dma_ring(u32 base, u32& voff, const void* sbase) {
  asm volatile("s_add_u32 m0, %1, %3\n\tv_add_u32 %0, %4, %0\n\tglobal_load_lds_dwordx4 %0, %2 sc1" : "+v"(voff) : "s"(base), "s"(sbase), "n"(IMM), "n"(STEP) : "memory", "scc");
}
DI void dma_ring2(u32 voff, const void* sbase) { asm volatile("global_load_lds_dwordx4 %0, %1 offset:1024 sc1" ::"v"(voff), "s"(sbase) : "memory"); }
DI float max3(float a, float b, float c) { float d; asm("v_max3_f32 %0, %1, %2, %3" : "=v"(d) : "v"(a), "v"(b), "v"(c)); return d; }
DI float nmin3(float a, float b, float c) { float d; asm("v_max3_f32 %0, -%1, -%2, -%3" : "=v"(d) : "v"(a), "v"(b), "v"(c)); return d; }
DI float nmin3m(float m, float b, float c) { float d; asm("v_max3_f32 %0, %1, -%2, -%3" : "=v"(d) : "v"(m), "v"(b), "v"(c)); return d; }
template <int OFF> DI void st16o(void* sbase, u32 voff, const u32x4& v) {
  asm volatile("global_store_dwordx4 %0, %1, %2 offset:%3 sc1\n\ts_nop 1" ::"v"(voff), "v"(v), "s"(sbase), "n"(OFF) : "memory");
}
DI void touch(const void* p, u32 lds) {
  asm volatile("s_mov_b32 m0, %1\n\ts_nop 0\n\tglobal_load_lds_dword %0, off" ::"v"(p), "s"(__builtin_amdgcn_readfirstlane(lds)) : "memory");
}
template <int N> DI void lgw() { asm volatile("s_waitcnt lgkmcnt(%0)" ::"n"(N) : "memory"); }
DI f32x2 pkfma(f32x2 a, f32x2 b, f32x2 c) { return __builtin_elementwise_fma(a, b, c); }
DI f32x2 pr(const f32x16& v, int i) { return (f32x2){v[i], v[i + 1]}; }
DI f32x2 sp2(float a) { return (f32x2){a, a}; }
template <int N> DI void vmwait() { asm volatile("s_waitcnt vmcnt(%0)" ::"n"(N) : "memory"); }
DI void lgwait() { asm volatile("s_waitcnt lgkmcnt(0)" ::: "memory"); }
DI void bar() { asm volatile("s_barrier" ::: "memory"); }
DI void st16d(void* sbase, u32 voff, const u32x4& v) { asm volatile("global_store_dwordx4 %0, %1, %2 sc1\n\ts_nop 1" ::"v"(voff), "v"(v), "s"(sbase) : "memory"); }
DI void st8d(void* sbase, u32 voff, const u32x2& v) { asm volatile("global_store_dwordx2 %0, %1, %2 sc1\n\ts_nop 1" ::"v"(voff), "v"(v), "s"(sbase) : "memory"); }
DI void st4d(void* sbase, u32 voff, u32 v) { asm volatile("global_store_dword %0, %1, %2 sc1\n\ts_nop 1" ::"v"(voff), "v"(v), "s"(sbase) : "memory"); }
DI void ld16(u32x4& d, const void* sbase, u32 voff) { asm volatile("global_load_dwordx4 %0, %1, %2" : "+v"(d) : "v"(voff), "s"(sbase) : "memory"); }
DI void ld16d(u32x4& d, const void* sbase, u32 voff) { asm volatile("global_load_dwordx4 %0, %1, %2 sc1" : "+v"(d) : "v"(voff), "s"(sbase) : "memory"); }
DI void ld8d(u32x2& d, const void* sbase, u32 voff) { asm volatile("global_load_dwordx2 %0, %1, %2 sc1" : "=v"(d) : "v"(voff), "s"(sbase) : "memory"); }
template <int OFF> DI void trd(u32x2& d, u32 a) { asm volatile("ds_read_b64_tr_b16 %0, %1 offset:%2" : "=v"(d) : "v"(a), "n"(OFF) : "memory"); }
template <int OFF> DI void rd128(u32x4& d, u32 a) { asm volatile("ds_read_b128 %0, %1 offset:%2" : "=v"(d) : "v"(a), "n"(OFF) : "memory"); }
DI u32x4 cat(const u32x2& a, const u32x2& b) { return (u32x4){a[0], a[1], b[0], b[1]}; }
DI void amfma(f32x16& c, const u32x4& a, const u32x4& b) { asm volatile("v_mfma_f32_32x32x16_bf16 %0, %1, %2, %0" : "+v"(c) : "v"(a), "v"(b)); }
DI void amfma16(f32x4& c, const u32x4& a, const u32x4& b) { asm volatile("v_mfma_f32_16x16x32_bf16 %0, %1, %2, %0" : "+v"(c) : "v"(a), "v"(b)); }
DI void amfma0(f32x16& c, const u32x4& a, const u32x4& b) { asm volatile("v_mfma_f32_32x32x16_bf16 %0, %1, %2, 0" : "=&v"(c) : "v"(a), "v"(b)); }
DI void amfma_n(f32x16& c, const u32x4& a, const u32x4& b) { asm volatile("s_nop 1\n\tv_mfma_f32_32x32x16_bf16 %0, %1, %2, %0" : "+v"(c) : "v"(a), "v"(b)); }

DI bf16x8 tr_frag(LAS char* p0, LAS char* p1) {
  s16x4 a = __builtin_amdgcn_ds_read_tr16_b64_v4i16((LAS s16x4*)p0);
  s16x4 b = __builtin_amdgcn_ds_read_tr16_b64_v4i16((LAS s16x4*)p1);
  return BF8(__builtin_shufflevector(a, b, 0, 1, 2, 3, 4, 5, 6, 7));
}
DI u32 pk2(float a, float b) { f32x2 v = {a, b}; return __builtin_bit_cast(u32, __builtin_convertvector(v, bf16x2)); }
DI u32 pkv(f32x2 v) { return __builtin_bit_cast(u32, __builtin_convertvector(v, bf16x2)); }
DI float bfr(float a) { return __uint_as_float(pk2(a, a) & 0xffff0000u); }
DI float lo_f(u32 w) { return __uint_as_float(w << 16); }
DI float hi_f(u32 w) { return __uint_as_float(w & 0xffff0000u); }
DI float fexp2(float x) { return __builtin_amdgcn_exp2f(x); }
DI float frcp(float x) { return __builtin_amdgcn_rcpf(x); }
DI void swap32(float& a, float& b) {
  auto r = __builtin_amdgcn_permlane32_swap(__float_as_uint(a), __float_as_uint(b), false, false);
  a = __uint_as_float(r[0]); b = __uint_as_float(r[1]);
}
template <int C> DI float dpp_add(float a) { return a + __uint_as_float(__builtin_amdgcn_update_dpp(0, __float_as_uint(a), C, 0xf, 0xf, true)); }
DI float wave_sum(float a) {
  a = dpp_add<0xB1>(a); a = dpp_add<0x4E>(a); a = dpp_add<0x141>(a); a = dpp_add<0x140>(a);
  auto r = __builtin_amdgcn_permlane16_swap(__float_as_uint(a), __float_as_uint(a), false, false);
  a = __uint_as_float(r[0]) + __uint_as_float(r[1]);
  float b = a; swap32(a, b);
  return a + b;
}
DI void poll8(const void* sbase, u32 voff, u32x2 tag) {
#pragma unroll 1
  for (u32 spin = 0; spin < SPIN_CAP; spin++) {
    u32x2 v;
    asm volatile("global_load_dwordx2 %0, %1, %2 sc1\n\ts_waitcnt vmcnt(0)" : "=&v"(v) : "v"(voff), "s"(sbase) : "memory");
    if (__all(v[0] == tag[0] && v[1] == tag[1])) break;
    __builtin_amdgcn_s_sleep(2);
  }
}

DI void poll_all(const void* sbase, u32 voff, u32x2 tag) {
#pragma unroll 1
  for (u32 spin = 0; spin < SPIN_CAP; spin++) {
    u32x2 a, b, c, d;
    asm volatile("global_load_dwordx2 %0, %4, %6 sc1\n\tglobal_load_dwordx2 %1, %4, %7 sc1\n\tglobal_load_dwordx2 %2, %5, %6 sc1\n\tglobal_load_dwordx2 %3, %5, %7 sc1\n\ts_waitcnt vmcnt(0)"
                 : "=&v"(a), "=&v"(b), "=&v"(c), "=&v"(d) : "v"(voff), "v"(voff + 8192), "s"(sbase), "s"((const char*)sbase + 4096) : "memory");
    const u32 x = ((a[0] ^ tag[0]) | (a[1] ^ tag[1]) | (b[0] ^ tag[0])) | ((b[1] ^ tag[1]) | (c[0] ^ tag[0]) | (c[1] ^ tag[1])) | ((d[0] ^ tag[0]) | (d[1] ^ tag[1]));
    if (__all(x == 0)) break;
    __builtin_amdgcn_s_sleep(2);
  }
}

constexpr int UPO = 6176, QO = 12352, WS1 = 14464, XR = 7 * WS1, XSLOT = 16384;
constexpr int P0R = 5 * WS1;
constexpr int VIMG = XR + 3 * XSLOT, STAT = VIMG + 8192, WPHI = STAT + 1024, JUNK = WPHI + 1024;
constexpr int SLOT2 = 12288, WO_L = 8 * SLOT2, REST_L = 6 * SLOT2, LDSB = 163840;
constexpr int R1F = 38;
DI constexpr int piece_l(int n) {
  return n < 84 ? ((n / 14 + 2) % 7) * WS1 + (n % 14 < 6 ? (n % 14) * 1024 : n % 14 < 12 ? UPO + (n % 14 - 6) * 1024 : QO + (n % 14 - 12) * 1024)
       : n < 116 ? XR + XSLOT + (n - 84) * 1024 : n < 128 ? WS1 + (n - 116) * 1024 : n < 144 ? XR + (n - 128) * 1024 : VIMG + (n - 144) * 1024;
}
DI int grp_l(int G) { return G < 3 ? WS1 + G * 4096 : G < 7 ? XR + (G - 3) * 4096 : VIMG + (G - 7) * 4096; }
DI void dma_grp(const char* src, u32 lds, const u32 (&vq)[4]) {
  asm volatile("s_mov_b32 m0, %0\n\ts_nop 0" ::"s"(__builtin_amdgcn_readfirstlane(lds)) : "memory");
  dma_p<0>(vq[0], src); dma_p<1>(vq[1], src); dma_p<2>(vq[2], src); dma_p<3>(vq[3], src);
}
struct Ctx {
  const char *xt, *wdc;
  const char *wqkv, *wphi, *wg, *wu, *wd, *wo;
  int np, ti, jc, p0;
  u32x2 tag;
  const char* code; u32 clim;
};
DI u32 two_bf(float v) { const float hi = bfr(v); return pk2(hi, v - hi); }

template <bool NORM>
DI void dual_softmax(const f32x16& p, float sc, u32 (&fp)[8], u32 (&fm)[8]) {
  const float mx = max3(max3(max3(p[0], p[1], p[2]), max3(p[3], p[4], p[5]), max3(p[6], p[7], p[8])),
                        max3(max3(p[9], p[10], p[11]), p[12], p[13]), max3(p[14], p[15], p[15])) * -sc;
  const float nn = max3(max3(nmin3(p[0], p[1], p[2]), nmin3(p[3], p[4], p[5]), nmin3(p[6], p[7], p[8])),
                        nmin3m(nmin3(p[9], p[10], p[11]), p[12], p[13]), nmin3(p[14], p[15], p[15])) * -sc;
  f32x2 ep[8], em[8];
#pragma unroll
  for (int j = 0; j < 8; j++) { ep[j] = pkfma(pr(p, 2 * j), sp2(sc), sp2(mx)); em[j] = pkfma(pr(p, 2 * j), sp2(-sc), sp2(nn)); }
#pragma unroll
  for (int j = 0; j < 8; j++) {
    ep[j] = (f32x2){fexp2(ep[j][0]), fexp2(ep[j][1])};
    em[j] = (f32x2){fexp2(em[j][0]), fexp2(em[j][1])};
  }
  const f32x2 tp = ((ep[0] + ep[1]) + (ep[2] + ep[3])) + ((ep[4] + ep[5]) + (ep[6] + ep[7]));
  const f32x2 tm = ((em[0] + em[1]) + (em[2] + em[3])) + ((em[4] + em[5]) + (em[6] + em[7]));
  float sp = tp[0] + tp[1], sm = frcp(tm[0] + tm[1]);
  if (NORM) sp = frcp(sp); else sm *= sp;
#pragma unroll
  for (int j = 0; j < 8; j++) {
    if (NORM) ep[j] *= sp2(sp);
    fp[j] = pkv(ep[j]); fm[j] = pkv(em[j] * sp2(sm));
  }
}

template <int ROLE, int NT>
DI void stage1(LAS char* sm, u32 lb, Ctx& c, char* ws, int w, int lane) {
  constexpr bool PG = ROLE != 0;
  const int tg = w & 3;
  const int r = lane & 31, h = lane >> 5, g16 = (lane >> 4) & 1, q4 = (lane & 15) >> 2, pp = lane & 3;
  const u32 vlin = lane * 16;
  u32 voff[4];
  const char *src, *cur = nullptr;
  u32 lds, sstep = 0;
  if constexpr (ROLE == 2) {
    const int rr = lane >> 3, pos = lane & 7, f = lane >> 4;
    voff[0] = rr * 1024 + 16 * (pos ^ f);
    voff[1] = rr * 1024 + 16 * (pos ^ f ^ 4) + 7168;
    voff[2] = voff[0] + 14336; voff[3] = voff[1] + 14336;
    src = c.xt + (w - 6) * 65536;
    lds = lb + XR + (w - 6) * 8192;
  } else if constexpr (ROLE == 1) {
#pragma unroll
    for (int j = 0; j < 4; j++) voff[j] = (lane >> 2) * 3072 + (lane & 3) * 16 + j * (16 * 3072 - 1024);
    src = c.wqkv + (w - 3) * 1024 + c.jc * 64;
    lds = lb + P0R + (w - 4) * 4096;
  }
  u64 ep;
  asm volatile("s_load_dwordx2 %0, %1, 0x0" : "=s"(ep) : "s"(ws + WS_EP) : "memory");
  auto quad = [&] { dma_p<0>(voff[0], cur); dma_p<1>(voff[1], cur); dma_p<2>(voff[2], cur); dma_p<3>(voff[3], cur); };
  auto xset = [&](auto sl_, auto hf_) {
    constexpr int sl = decltype(sl_)::value, hf = decltype(hf_)::value;
    set_m0<(sl % 3) * XSLOT + hf * 4096>(lds);
    cur = src + ((sl & 7) * 128 + hf * 32768);
  };
  auto kvset = [&](auto s_) { constexpr int s = decltype(s_)::value; set_m0<(s % 3) * 8192>(lds); cur = src + s * (64 * 3072); };
  u32x4 srow;
  auto ld_srow = [&] { u32x4 t; asm volatile("global_load_dwordx4 %0, %1, %2" : "=v"(t) : "v"(vlin), "s"(c.xt + (8 * c.jc + w) * 1024) : "memory"); srow = t; };
  auto lines = [&](int which) {
    const int b = blockIdx.x;
    const char* p;
    if (which == 0) p = c.wqkv + (b * 48 + min(lane, 47)) * 128;
    else if (which < 3) { const int g = b * 43 + min(lane, 42); p = (g < 5504 ? c.wg + g * 128 : c.wu + (g - 5504) * 128) + (which - 1) * 704512; }
    else { const int g = b * 59 + min(lane, 58); p = g < 11008 ? c.wd + g * 128 : c.wo + (g - 11008) * 128; }
    touch(p, lb + JUNK + 1024);
  };
  auto code = [&](int half) { dma16(c.code, min((blockIdx.x >> 3) * 1024 + vlin + half * 32768, c.clim), lb + JUNK); };
  if constexpr (ROLE == 2) {
    lds = __builtin_amdgcn_readfirstlane(lds);
    xset(IC<0>{}, IC<0>{}); asm volatile("s_nop 0"); quad(); xset(IC<0>{}, IC<1>{}); asm volatile("s_nop 0"); quad();
    ld_srow();
    xset(IC<1>{}, IC<0>{}); asm volatile("s_nop 0"); quad(); xset(IC<1>{}, IC<1>{}); asm volatile("s_nop 0"); quad();
  } else if constexpr (ROLE == 1) {
    lds = __builtin_amdgcn_readfirstlane(lds);
    kvset(IC<0>{}); asm volatile("s_nop 0"); quad();
    ld_srow();
    kvset(IC<1>{}); asm volatile("s_nop 0"); quad();
  } else {
    if (w == 0) dma16(c.wphi + c.jc * 1024, vlin, lb + WPHI);
    ld_srow();
    if (w == 0) lines(0); else code(w & 1);
    const int emax = 2 * c.np - 1;
#pragma unroll
    for (int m = 0; m < 3; m++) {
      const int ch = 64 * (3 * (w & 1) + m) + lane, row = (ch * 5462) >> 16;
      int e = ch - row * 12; e = e > emax ? emax : e;
      voff[m] = (u32)(row * (2 * FF) + 16 * e) - m * 1024;
    }
    voff[3] = 0;
    src = ((w >> 1) ? c.wu : c.wg) + c.p0 * 32;
    lds = __builtin_amdgcn_readfirstlane(lb + (w >> 1) * UPO + (w & 1) * 3072);
    sstep = 64 * FF;
  }
  const u32 vw = (lane >> 3) * 1024 + 16 * ((lane & 7) ^ (((lane >> 4) & 1) << 2));
  u32 xa[4], ka = lb + P0R + PG * 4096 + (8 * h + q4) * 64 + 32 * g16 + 8 * pp, gaddr = r * 16;
#pragma unroll
  for (int k = 0; k < 4; k++) { xa[k] = (lb + XR + (32 * tg + r) * 128 + 16 * (h ^ ((r >> 1) & 7))) ^ (32 * k); asm volatile("" : "+v"(xa[k])); }
  f32x16 acc[NT], zero;
#pragma unroll
  for (int j = 0; j < 16; j++) zero[j] = 0.f;
  acc[0] = zero;
  f32x4 cs0 = {0.f, 0.f, 0.f, 0.f}, cs1 = cs0;
  const u32 one = ((lane & 31) == 0 || (lane & 31) == 20) ? 0x3f803f80u : 0u;
  u32x4 sel = {one, one, one, one};
  asm volatile("s_waitcnt lgkmcnt(0)" : "+s"(ep), "+v"(cs0), "+v"(cs1), "+v"(sel), "+v"(gaddr), "+v"(ka), "+v"(acc[0]) :: "memory");
  ep += 1;
  c.tag = (u32x2){(u32)ep, (u32)(ep >> 32)};
  u32x2 A[2][NT][2];
  u32x4 B[2], gran;
  auto rdx = [&](auto k_) { constexpr int k = decltype(k_)::value; rd128<((k >> 2) % 3) * XSLOT>(B[k & 1], xa[k & 3]); };
  auto rdk = [&](auto k_) {
    constexpr int k = decltype(k_)::value, O = ((k >> 2) % 3) * 8192 + (k & 3) * 1024;
    trd<O>(A[k & 1][0][0], ka); trd<O + 256>(A[k & 1][0][1], ka);
  };
  auto wset = [&](auto u_) {
    constexpr int U = decltype(u_)::value;
    if constexpr (U == 16) {
      src = c.wdc + (ROLE == 0 ? 3 * w : 8 + w) * 8192; sstep = 14 * 8192;
      voff[0] = vw; voff[1] = vw + 7168; voff[2] = vw + 14336;
    }
    set_m0<(U % 7) * WS1>(lds); cur = src; src += sstep;
  };
  auto p0_pieces = [&](auto k_) {
    constexpr int k = decltype(k_)::value, sl = ((k + 1) >> 2) + 2, j = (k + 1) & 3;
    if constexpr (ROLE == 2) {
      if constexpr (k <= 30) {
        if constexpr (!(j & 1)) { xset(IC<sl>{}, IC<(j >> 1)>{}); asm volatile("s_nop 0"); }
        dma_p<2 * (j & 1)>(voff[2 * (j & 1)], cur); dma_p<2 * (j & 1) + 1>(voff[2 * (j & 1) + 1], cur);
      }
    } else if constexpr (ROLE == 1) {
      if constexpr (k <= 22) {
        if constexpr (j == 0) { kvset(IC<sl>{}); asm volatile("s_nop 0"); }
        dma_p<j>(voff[j], cur);
      } else if constexpr (k >= 27) {
        if constexpr (k == 27) {
          src = c.wqkv + c.jc * 64 + (w - 4) * (16 * 3072); sstep = 32 * 3072;
          lds = __builtin_amdgcn_readfirstlane(lb + QO + (w - 4) * 1024);
        }
        wset(IC<k - 27>{}); asm volatile("s_nop 0"); dma_p<0>(voff[0], cur);
      }
    } else if constexpr (k >= 17) {
      if constexpr ((k - 17) % 3 == 0) { wset(IC<(k - 17) / 3>{}); asm volatile("s_nop 0"); }
      dma_p<(k - 17) % 3>(voff[(k - 17) % 3], cur);
    }
  };
  if constexpr (ROLE == 2) vmwait<9>(); else if constexpr (ROLE == 1) vmwait<5>();
  bar();
  STAMP(1);
  rdx(IC<0>{}); rdk(IC<0>{});
  p0_pieces(IC<-1>{});
  sfor<0, 32>([&](auto k_) {
    constexpr int k = decltype(k_)::value, C = k & 1;
    lgwait();
    amfma(acc[0], cat(A[C][0][0], A[C][0][1]), B[C]);
    if constexpr ((k & 3) == 3 && k < 31) {
      if constexpr (ROLE == 2) vmwait<8>(); else if constexpr (ROLE == 1) vmwait<(k == 27 ? 1 : 4)>();
      bar();
    }
    if constexpr (k < 31) { rdx(IC<k + 1>{}); rdk(IC<k + 1>{}); }
    if constexpr (k == 23) { u32x4 t; asm volatile("global_load_dwordx4 %0, %1, %2 sc1" : "=v"(t) : "v"(gaddr), "s"(ws + WS_RS + (128 * c.ti + 32 * tg) * 16) : "memory"); gran = t; }
    if constexpr (ROLE == 0 && (k == 7 || k == 13)) { if (w == (k == 7 ? 2 : 3)) lines(k == 7 ? 1 : 2); }
    p0_pieces(k_);
    amfma16(cs0, sel, cat(A[C][0][0], A[C][0][1]));
    if constexpr (k == 4) {
      if constexpr (ROLE == 0) vmwait<1>();
      asm volatile("" : "+v"(srow));
      const float piv = lo_f(__builtin_amdgcn_readfirstlane(srow[0]));
      f32x2 v[4];
#pragma unroll
      for (int j = 0; j < 4; j++) v[j] = (f32x2){lo_f(srow[j]), hi_f(srow[j])} - sp2(piv);
      const f32x2 s2 = (v[0] + v[1]) + (v[2] + v[3]), q2 = pkfma(v[0], v[0], v[1] * v[1]) + pkfma(v[2], v[2], v[3] * v[3]);
      const float ms = wave_sum(s2[0] + s2[1]) * (1.f / 512.f), mq = wave_sum(q2[0] + q2[1]) * (1.f / 512.f);
      const float mean = piv + ms, var = fmaxf(mq - ms * ms, 0.f);
      const float sd = bfr(__builtin_amdgcn_sqrtf(bfr(bfr(var) + 1e-5f)));
      st16d(ws + WS_RS + (128 * c.ti + 8 * c.jc + w) * 16, 0u, (u32x4){__float_as_uint(sd), __float_as_uint(bfr(mean)), c.tag[0], c.tag[1]});
    }
  });
  vmwait<(ROLE == 0 ? 9 : ROLE == 1 ? 5 : 16)>();
  asm volatile("s_nop 15" : "+v"(cs0), "+v"(gran), "+v"(acc[0]) :: "memory");
  STAMP(2);
#pragma unroll 1
  for (u32 spin = 0; spin < SPIN_CAP && !__all(gran[2] == c.tag[0] && gran[3] == c.tag[1]); spin++) {
    __builtin_amdgcn_s_sleep(2);
    asm volatile("global_load_dwordx4 %0, %1, %2 sc1\n\ts_waitcnt vmcnt(0)" : "=&v"(gran) : "v"(gaddr), "s"(ws + WS_RS + (128 * c.ti + 32 * tg) * 16) : "memory");
  }
  const float sd = __uint_as_float(gran[0]), rs = frcp(sd), sc = rs * 1.4426950408889634f;
  const u32x4 mfrag = {h ? 0u : ((gran[1] >> 16) ^ 0x8000u) * 0x10001u, 0u, 0u, 0u};
  const bool b2 = (r >> 2) & 1;
  auto phi = [&](const f32x16& a) {
    LAS char* wp = sm + WPHI + (pp & 1) * 512 + (4 * h + q4) * 32 + 8 * (2 * g16 + (pp >> 1));
    const u32x4 wv = __builtin_bit_cast(u32x4, tr_frag(wp, wp + 256));
    u32x4 wA, wB, qa, qb;
#pragma unroll
    for (int j = 0; j < 4; j++) {
      wA[j] = b2 ? 0u : wv[j]; wB[j] = b2 ? wv[j] : 0u;
      qa[j] = pkv(pr(a, 2 * j)); qb[j] = pkv(pr(a, 8 + 2 * j));
    }
    f32x16 P = MFMA32(BF8(wB), BF8(qb), MFMA32(BF8(wA), BF8(qa), zero));
    asm volatile("s_nop 15" : "+v"(P));
    return P;
  };
  acc[0] = MFMA32(BF8(((u32x4){two_bf(cs0[0]), 0u, 0u, 0u})), BF8(mfrag), acc[0]);
  {
    const int tok = 32 * tg + r;
    if constexpr (ROLE == 0) {
      u32 fp[8], fm[8];
      dual_softmax<true>(phi(acc[0]), sc, fp, fm);
      LAS char* pd = sm + P0R + h * 16384 + tok * 64;
      const int fsw = (r >> 1) & 7;
#pragma unroll
      for (int k = 0; k < 4; k++) {
        const int u = 2 * (k & 1) + (k >> 1);
        *(LAS u32x2*)(pd + 8 * (u ^ fsw)) = (u32x2){fp[2 * k], fp[2 * k + 1]};
        *(LAS u32x2*)(pd + 8 * ((4 + u) ^ fsw)) = (u32x2){fm[2 * k], fm[2 * k + 1]};
      }
    } else {
      LAS char* vd = sm + VIMG + tok * 32;
      const int vsw = (r >> 2) & 3;
#pragma unroll
      for (int e = 0; e < 2; e++) {
        *(LAS u32x2*)(vd + e * 4096 + 8 * ((0 + h) ^ vsw)) = (u32x2){pkv(pr(acc[0], 8 * e) * sp2(rs)), pkv(pr(acc[0], 8 * e + 2) * sp2(rs))};
        *(LAS u32x2*)(vd + e * 4096 + 8 * ((2 + h) ^ vsw)) = (u32x2){pkv(pr(acc[0], 8 * e + 4) * sp2(rs)), pkv(pr(acc[0], 8 * e + 6) * sp2(rs))};
      }
    }
  }
  u32 wl[NT], wh[NT];
#pragma unroll
  for (int s = 0; s < NT; s++) {
    int t = s + tg;
    if constexpr (PG) t = c.np - 2 + (t & 1); else t = t >= NT ? t - NT : t;
    wl[s] = (PG && s == 2) ? lb + QO + (8 * h + q4) * 64 + 32 * g16 + 8 * pp : lb + g16 * UPO + (8 * h + q4) * 192 + 8 * pp + 32 * t;
    wh[s] = wl[s] + 4 * WS1;
    acc[s] = zero;
    asm volatile("" : "+v"(acc[s]), "+v"(wl[s]), "+v"(wh[s]));
  }
  cs0 = (f32x4){0.f, 0.f, 0.f, 0.f};
  asm volatile("" : "+v"(cs0));
  lgwait();
  bar();
  STAMP(12);
  if constexpr (ROLE == 2) {
    xset(IC<10>{}, IC<0>{}); asm volatile("s_nop 0"); quad(); xset(IC<10>{}, IC<1>{}); asm volatile("s_nop 0"); quad();
  }
  if constexpr (ROLE == 1) {
    const int t0 = 8 * h + q4;
    const u32 pb = lb + P0R + (w - 4) * 16384, vb = lb + VIMG + (w - 4) * 4096;
    const u32 pa0 = pb + t0 * 64 + 8 * ((4 * g16 + pp) ^ (t0 >> 1)), pa1 = pb + (t0 + 4) * 64 + 8 * ((4 * g16 + pp) ^ ((t0 >> 1) + 2));
    const u32 pb0 = vb + t0 * 32 + 8 * (pp ^ (2 * h)), pb1 = vb + (t0 + 4) * 32 + 8 * (pp ^ (2 * h + 1));
    u32x2 fa[8][2], fb[8][2];
    sfor<0, 8>([&](auto k_) {
      constexpr int k = decltype(k_)::value;
      trd<k * 1024>(fa[k][0], pa0); trd<k * 1024>(fa[k][1], pa1); trd<k * 512>(fb[k][0], pb0); trd<k * 512>(fb[k][1], pb1);
    });
    f32x16 kv = zero;
    sfor<0, 8>([&](auto k_) {
      constexpr int k = decltype(k_)::value;
      if constexpr (k == 0) lgw<15>();
      if constexpr (k == 4) lgwait();
      asm volatile("" : "+v"(fa[k][0]), "+v"(fa[k][1]), "+v"(fb[k][0]), "+v"(fb[k][1]));
      u32x4 bq = cat(fb[k][0], fb[k][1]);
#pragma unroll
      for (int j = 0; j < 4; j++) bq[j] = g16 ? 0x3f803f80u : bq[j];
      kv = MFMA32(BF8(cat(fa[k][0], fa[k][1])), BF8(bq), kv);
    });
    char* dst = ws + WS_KVP + (size_t)(c.ti * 32 + 2 * c.jc + w - 4) * 8192;
    const u32 vo = (r > 16 ? (lane & 32) + 16 : lane) * 16;
    sfor<0, 8>([&](auto p_) {
      constexpr int p = decltype(p_)::value;
      st16o<(p & 3) * 1024>(dst + (p >> 2) * 4096, vo, (u32x4){__float_as_uint(kv[2 * p]), __float_as_uint(kv[2 * p + 1]), c.tag[0], c.tag[1]});
    });
  }
  auto rdw = [&](auto k_, auto s_) {
    constexpr int k = decltype(k_)::value, s = decltype(s_)::value, sl = (k >> 1) % 7, Q = PG && s == 2;
    constexpr int O = (sl & 3) * WS1 + (k & 1) * (Q ? 1024 : 3072);
    trd<O>(A[k & 1][s][0], sl < 4 ? wl[s] : wh[s]); trd<O + (Q ? 256 : 768)>(A[k & 1][s][1], sl < 4 ? wl[s] : wh[s]);
  };
  auto p1_set = [&](auto v_) {
    constexpr int v = decltype(v_)::value;
    if constexpr (ROLE == 2) {
      if constexpr (v == 12 || v == 14) {
        src = c.wdc + ((v == 12 ? 84 : 100) + (w - 6) * 8) * 8192;
#pragma unroll
        for (int j = 0; j < 4; j++) voff[j] = vw + j * 7168;
      }
      if constexpr (v >= 12) { set_m0<((10 + (v >> 1)) % 3) * XSLOT + (v & 1) * 4096>(lds); cur = src + (v & 1) * 32768; }
      else if constexpr (v >= 2) xset(IC<10 + (v >> 1)>{}, IC<(v & 1)>{});
    } else wset(IC<v + 6>{});
  };
  auto p1_piece = [&](auto v_, auto j_) {
    constexpr int v = decltype(v_)::value, j = decltype(j_)::value;
    if constexpr (v <= 15 && (ROLE == 2 ? v >= 2 : j < (ROLE == 0 ? 3 : 1))) dma_p<j>(voff[j], cur);
  };
  vmwait<(ROLE == 2 ? 16 : 12)>();
  bar();
  STAMP(13);
  rdx(IC<32>{});
  sfor<0, NT>([&](auto s_) { rdw(IC<0>{}, s_); });
  if constexpr (ROLE != 2) {
    wset(IC<5>{}); asm volatile("s_nop 0"); sfor<0, 3>([&](auto j_) { p1_piece(IC<0>{}, j_); });
    wset(IC<6>{}); asm volatile("s_nop 0"); p1_piece(IC<0>{}, IC<0>{}); p1_piece(IC<0>{}, IC<1>{});
  }
  u32x4 part[16], kvf[4];
  u32x2 f2;
  const int khead = 2 * c.jc + (c.ti >> 3), kp = c.ti & 7;
  const char* psrc = ws + WS_KVP + khead * 8192 + kp * 1024;
  const u32 pvo = (r > 16 ? (lane & 32) + 16 : lane) * 16;
  sfor<0, 32>([&](auto k_) {
    constexpr int k = decltype(k_)::value, C = k & 1, v = (k + 1) >> 1;
    constexpr bool N = k < 31;
    using K1 = IC<k + 1>;
    lgwait();
    amfma(acc[0], cat(A[C][0][0], A[C][0][1]), B[C]);
    if constexpr (C && v < 16) {
      if constexpr (ROLE == 0) vmwait<15>();
      else if constexpr (ROLE == 1) vmwait<(v <= 4 ? 13 : v <= 6 ? 21 : 5)>();
      else if constexpr (!(v & 1)) vmwait<8>();
      bar();
      if constexpr (ROLE == 0 && v == 3) { if (w == 0) lines(3); }
      if constexpr (ROLE == 0 && v == 9) { if (w >= 2) code(w & 1); }
      if constexpr (ROLE == 1 && v == 6) {
        vmwait<1>();
        u32 bad = 0;
#pragma unroll
        for (int i = 0; i < 16; i++) { asm volatile("" : "+v"(part[i])); bad |= (part[i][2] ^ c.tag[0]) | (part[i][3] ^ c.tag[1]); }
#pragma unroll 1
        for (u32 spin = 0; spin < SPIN_CAP && !__all(bad == 0); spin++) {
          __builtin_amdgcn_s_sleep(2);
#pragma unroll
          for (int i = 0; i < 16; i++) ld16d(part[i], psrc + (size_t)i * 32 * 8192, pvo);
          vmwait<0>();
          bad = 0;
#pragma unroll
          for (int i = 0; i < 16; i++) { asm volatile("" : "+v"(part[i])); bad |= (part[i][2] ^ c.tag[0]) | (part[i][3] ^ c.tag[1]); }
        }
        f32x2 s2 = {0.f, 0.f};
#pragma unroll
        for (int i = 0; i < 16; i++) s2 += (f32x2){__uint_as_float(part[i][0]), __uint_as_float(part[i][1])};
        if (r >= 16 && kp < 4) s2 += sp2(1e-6f);
        st4d(ws + WS_KVF + khead * 2048 + (kp >> 2) * 1024 + (kp & 3) * 4, vlin, pkv(s2));
      }
      if constexpr (ROLE == 1 && v == 8) { vmwait<2>(); st8d(ws + WS_F2 + c.jc * 128 + c.ti * 8, 0u, c.tag); }
      if constexpr (PG && v == 13) ld8d(f2, ws + WS_F2 + c.jc * 128, (lane & 15) * 8);
      if constexpr (PG && v == 15) {
        vmwait<(ROLE == 1 ? 2 : 8)>();
        asm volatile("" : "+v"(f2));
        if (!__all(f2[0] == c.tag[0] && f2[1] == c.tag[1])) poll8(ws + WS_F2 + c.jc * 128, (lane & 15) * 8, c.tag);
        const char* ks = ws + WS_KVF + c.jc * 4096;
        u32x4 t0, t1, t2, t3;
        asm volatile("global_load_dwordx4 %0, %4, %5 sc1\n\tglobal_load_dwordx4 %1, %4, %5 offset:1024 sc1\n\tglobal_load_dwordx4 %2, %4, %5 offset:2048 sc1\n\tglobal_load_dwordx4 %3, %4, %5 offset:3072 sc1"
                     : "=&v"(t0), "=&v"(t1), "=&v"(t2), "=&v"(t3) : "v"(vlin), "s"(ks) : "memory");
        kvf[0] = t0; kvf[1] = t1; kvf[2] = t2; kvf[3] = t3;
      }
      p1_set(IC<v>{});
    }
    if constexpr (N) { rdx(IC<k + 33>{}); rdw(K1{}, IC<0>{}); }
    p1_piece(IC<v>{}, IC<(C ? 0 : 2)>{});
    if constexpr (ROLE == 1 && k == 7) {
#pragma unroll
      for (int i = 0; i < 16; i++) { u32x4 t; asm volatile("global_load_dwordx4 %0, %1, %2 sc1" : "=v"(t) : "v"(pvo), "s"(psrc + (size_t)i * 32 * 8192) : "memory"); part[i] = t; }
    }
    amfma(acc[1], cat(A[C][1][0], A[C][1][1]), B[C]);
    if constexpr (N) { rdw(K1{}, IC<1>{}); rdw(K1{}, IC<2>{}); }
    amfma16(cs0, sel, cat(A[C][0][0], A[C][0][1]));
    amfma(acc[2], cat(A[C][2][0], A[C][2][1]), B[C]);
    if constexpr (N && NT == 4) rdw(K1{}, IC<NT - 1>{});
    p1_piece(IC<v>{}, IC<(C ? 1 : 3)>{});
    if constexpr (PG) amfma16(cs1, sel, cat(A[C][2][0], A[C][2][1]));
    if constexpr (NT == 4) amfma(acc[NT - 1], cat(A[C][NT - 1][0], A[C][NT - 1][1]), B[C]);
  });
  asm volatile("s_nop 15" : "+v"(cs0), "+v"(cs1) :: "memory");
#pragma unroll
  for (int s = 0; s < NT; s++) asm volatile("" : "+v"(acc[s]));
  STAMP(3);
  auto tile = [&](int s) { int t = s + tg; if constexpr (PG) t = c.np - 2 + (t & 1); else t = t >= NT ? t - NT : t; return t; };
  if (lane < 32) *(LAS u32*)(sm + STAT + (tile(0) * 32 + lane) * 4) = two_bf(cs0[0]);
  u32 vq[4];
#pragma unroll
  for (int j = 0; j < 4; j++) vq[j] = vw + j * 7168;
  if constexpr (PG) acc[2] = MFMA32(BF8(((u32x4){two_bf(cs1[0]), 0u, 0u, 0u})), BF8(mfrag), acc[2]);
  lgwait();
  bar();
#pragma unroll
  for (int s = 0; s < (PG ? 2 : NT); s++) {
    const u32x4 cf = {*(LAS u32*)(sm + STAT + (tile(s) * 32 + r) * 4), 0u, 0u, 0u};
    acc[s] = MFMA32(BF8(cf), BF8(mfrag), acc[s]);
  }
  char* dst = ws + WS_H + (size_t)(4 * c.ti + tg) * (NKS * 1024);
  if constexpr (PG) {
    u32 fp[8], fm[8];
    dual_softmax<false>(phi(acc[2]), sc, fp, fm);
#pragma unroll
    for (int i = 0; i < 4; i++) {
      auto a = __builtin_amdgcn_permlane32_swap(fp[i], fp[i + 4], false, false); fp[i] = a[0]; fp[i + 4] = a[1];
      auto b = __builtin_amdgcn_permlane32_swap(fm[i], fm[i + 4], false, false); fm[i] = b[0]; fm[i + 4] = b[1];
    }
    vmwait<(ROLE == 1 ? 1 : 4)>();
    asm volatile("" : "+v"(kvf[0]), "+v"(kvf[1]), "+v"(kvf[2]), "+v"(kvf[3]));
#pragma unroll
    for (int e = 0; e < 2; e++) {
      f32x16 n = MFMA32(BF8(kvf[2 * e]), BF8(((u32x4){fp[4 * e], fp[4 * e + 1], fp[4 * e + 2], fp[4 * e + 3]})), zero);
      n = MFMA32(BF8(kvf[2 * e + 1]), BF8(((u32x4){fm[4 * e], fm[4 * e + 1], fm[4 * e + 2], fm[4 * e + 3]})), n);
      const f32x2 rd = sp2(frcp(n[8]));
      st16d(dst + (86 + 2 * c.jc + e) * 1024, vlin, (u32x4){pkv(pr(n, 0) * rd), pkv(pr(n, 2) * rd), pkv(pr(n, 4) * rd), pkv(pr(n, 6) * rd)});
    }
  }
  dma_grp(c.wdc + (116 + 4 * w) * 8192, lb + grp_l(w), vq);
  {
    const float K0 = -2.f * 0.7978845608028654f * 1.4426950408889634f * rs, K1 = K0 * 0.044715f * rs * rs, v2 = sd * sd;
#pragma unroll
    for (int s = 0; s < (PG ? 2 : NT); s++) {
      f32x2 e[4], gu[4];
#pragma unroll
      for (int j = 0; j < 4; j++) { const f32x2 g = pr(acc[s], 2 * j); e[j] = g * pkfma(g * g, sp2(K1), sp2(K0)); gu[j] = g * pr(acc[s], 8 + 2 * j); }
#pragma unroll
      for (int j = 0; j < 4; j++) e[j] = pkfma((f32x2){fexp2(e[j][0]), fexp2(e[j][1])}, sp2(v2), sp2(v2));
#pragma unroll
      for (int j = 0; j < 4; j++) gu[j] *= (f32x2){frcp(e[j][0]), frcp(e[j][1])};
      st16d(dst + c.p0 * 1024, vlin + tile(s) * 1024, (u32x4){pkv(gu[0]), pkv(gu[1]), pkv(gu[2]), pkv(gu[3])});
      if (ROLE == 0 && s == 0 && w == 0) dma_grp(c.wdc + 148 * 8192, lb + grp_l(8), vq);
    }
  }
}

constexpr int need_n(bool lo, int t) {
  const int issued = t == 0 ? 5 : (t + 6 > 19 ? 19 : t + 6);
  return (issued - t) * (lo ? 2 : 1);
}

template <bool HI>
DI void stage2(LAS char* sm, u32 lb, const Ctx& c, char* ws, int w, int lane, int ti2, int cj, const u16* x, u16* out) {
  const int tg = w & 1, cg = (w >> 1) & 1;
  constexpr int kh = HI;
  const int r = lane & 31, h = lane >> 5, g16 = (lane >> 4) & 1, q4 = (lane & 15) >> 2, pp = lane & 3;
  const u32 vlin = lane * 16;
  const u32 vw = (lane >> 3) * 1024 + 16 * ((lane & 7) ^ (((lane >> 4) & 1) << 2));
  const u32 wa0 = lb + (4 * h + q4) * 128 + ((cg ^ (q4 >> 1)) << 6) + 32 * g16 + 8 * pp, wa = wa0 + kh * 2048;
  u32x2 wf[43][2];
  sfor<0, R1F>([&](auto i) {
    constexpr int I = decltype(i)::value, n = 4 * I + 2 * kh, O0 = piece_l(n), O1 = piece_l(n + 1);
    trd<(O0 & 0xffff)>(wf[I][0], wa0 + (O0 & ~0xffff)); trd<(O1 & 0xffff)>(wf[I][1], wa0 + (O1 & ~0xffff));
  });
  lgwait();
  bar();
  STAMP(5);
  {
    u32 vq[4];
#pragma unroll
    for (int j = 0; j < 4; j++) vq[j] = vw + j * 7168;
    if (!HI && w == 0) poll8(ws + WS_F1 + (ti2 >> 1) * 1024, (lane & 15) * 64, c.tag);
    else if (w < 6) dma_grp(c.wdc + (148 + 4 * w) * 8192, lb + REST_L + (w - 1) * 4096, vq);
    const char* so = c.wo + w * 65536 + cj * 128;
    dma_grp(so, lb + WO_L + w * 8192, vq);
    dma_grp(so + 32768, lb + WO_L + w * 8192 + 4096, vq);
  }
  vmwait<8>();
  bar();
  STAMP(6);
  const size_t orow = (size_t)(64 * ti2 + 32 * tg + r) * DM + 64 * cj + 32 * cg + 16 * kh;
  const u32x2 xr0 = *(const u32x2*)(x + orow + 4 * h), xr1 = *(const u32x2*)(x + orow + 8 + 4 * h);
  f32x16 accD, accS;
#pragma unroll
  for (int j = 0; j < 16; j++) { accD[j] = 0.f; accS[j] = 0.f; }
  {
    const int p0 = HI ? w + 4 : 2 * w;
    const char* hS = ws + WS_H + (size_t)((2 * ti2 + (p0 >= 6)) * NKS + (p0 >= 6 ? p0 - 6 : p0)) * 1024;
    const u32 mh = __builtin_amdgcn_readfirstlane(lb + p0 * 1024);
    u32 vh = vlin - 6144;
    auto issue_a = [&](auto st_) { constexpr int st = decltype(st_)::value; if constexpr (st <= 19) dma_ring<(st & 7) * SLOT2, 6144>(mh, vh, hS); };
    auto issue_b = [&](auto st_) { constexpr int st = decltype(st_)::value; if constexpr (st <= 19 && !HI) dma_ring2(vh, hS); };
    sfor<0, 6>([&](auto t_) { issue_a(t_); issue_b(t_); });
    sfor<R1F, 43>([&](auto i) {
      constexpr int I = decltype(i)::value, O = (I - R1F) * 4096;
      trd<O>(wf[I][0], wa + REST_L); trd<O + 1024>(wf[I][1], wa + REST_L);
    });
    u32 ha[2], woa = wa + WO_L;
    ha[0] = lb + tg * 6144 + HI * 1024 + vlin; ha[1] = ha[0] + 4 * SLOT2;
    u32x4 hf[2][3];
    u32x2 of[2][3][2];
    asm volatile("" : "+v"(accD), "+v"(accS), "+v"(ha[0]), "+v"(ha[1]), "+v"(woa));
    auto mm = [&](auto p_, auto i_) {
      constexpr int p = decltype(p_)::value, i = decltype(i_)::value, I = 3 * p + i;
      if constexpr (p >= 0 && I < 43) amfma(accD, cat(wf[I][0], wf[I][1]), hf[p & 1][i]);
      else if constexpr (p >= 0 && I < 59) amfma(accS, cat(of[p & 1][i][0], of[p & 1][i][1]), hf[p & 1][i]);
    };
    sfor<0, 21>([&](auto t_) {
      constexpr int t = decltype(t_)::value;
      using P = IC<t - 1>;
      lgwait();
      mm(P{}, IC<0>{});
      if constexpr (t <= 19) {
        vmwait<need_n(!HI, t)>();
        bar();
        if constexpr (t == 0) STAMP(7);
        if constexpr (t == 14) STAMP(9);
        sfor<0, 3>([&](auto i_) {
          constexpr int i = decltype(i_)::value, I = 3 * t + i;
          if constexpr (I < 59) rd128<(t & 3) * SLOT2 + i * 2048>(hf[t & 1][i], ha[(t >> 2) & 1]);
          if constexpr (I >= 43 && I < 59) { trd<(I - 43) * 4096>(of[t & 1][i][0], woa); trd<(I - 43) * 4096 + 1024>(of[t & 1][i][1], woa); }
        });
      }
      mm(P{}, IC<1>{});
      if constexpr (t == 0) { issue_a(IC<6>{}); issue_b(IC<6>{}); }
      issue_a(IC<t + 7>{});
      mm(P{}, IC<2>{});
      issue_b(IC<t + 7>{});
      if constexpr (t == 19 && !HI) {
        if (blockIdx.x == 0 && w == 1) {
          poll_all(ws + WS_F1, lane * 64, c.tag);
          st8d(ws + WS_EP, 0u, c.tag);
        }
      }
    });
  }
  asm volatile("s_nop 15" : "+v"(accD), "+v"(accS));
  STAMP(10);
  {
    constexpr int m = 8 * kh, o = 8 - m;
    LAS f32x4* pd = (LAS f32x4*)(sm + 4 * SLOT2 + (w ^ 4) * 4096 + lane * 16);
    pd[0] = (f32x4){accD[o], accD[o + 1], accD[o + 2], accD[o + 3]}; pd[64] = (f32x4){accD[o + 4], accD[o + 5], accD[o + 6], accD[o + 7]};
    pd[128] = (f32x4){accS[o], accS[o + 1], accS[o + 2], accS[o + 3]}; pd[192] = (f32x4){accS[o + 4], accS[o + 5], accS[o + 6], accS[o + 7]};
    lgwait();
    bar();
    LAS f32x4* ps = (LAS f32x4*)(sm + 4 * SLOT2 + w * 4096 + lane * 16);
    const u32 xr[4] = {xr0[0], xr0[1], xr1[0], xr1[1]};
    u32 ov[4];
#pragma unroll
    for (int i = 0; i < 2; i++) {
      const f32x4 d = ps[64 * i] + (f32x4){accD[m + 4 * i], accD[m + 4 * i + 1], accD[m + 4 * i + 2], accD[m + 4 * i + 3]};
      const f32x4 sv = ps[128 + 64 * i] + (f32x4){accS[m + 4 * i], accS[m + 4 * i + 1], accS[m + 4 * i + 2], accS[m + 4 * i + 3]};
#pragma unroll
      for (int j = 0; j < 2; j++) {
        const f32x2 dd = {d[2 * j], d[2 * j + 1]}, z = dd * sp2(-1.4426950408889634f);
        const f32x2 e = (f32x2){fexp2(z[0]), fexp2(z[1])} + sp2(1.f);
        const f32x2 g = dd * (f32x2){frcp(e[0]), frcp(e[1])};
        ov[2 * i + j] = pkv(pkfma((f32x2){sv[2 * j], sv[2 * j + 1]}, g, (f32x2){lo_f(xr[2 * i + j]), hi_f(xr[2 * i + j])}));
      }
    }
    *(u32x2*)(out + orow + 4 * h) = (u32x2){ov[0], ov[1]};
    *(u32x2*)(out + orow + 8 + 4 * h) = (u32x2){ov[2], ov[3]};
  }
  if constexpr (HI) asm volatile(".Lfused_end1:"); else asm volatile(".Lfused_end0:");
  STAMP(11);
}

__global__ __launch_bounds__(512, 2) void fused(const u16* __restrict__ x, const u16* __restrict__ wqkv, const u16* __restrict__ wphi,
                                                const u16* __restrict__ wo, const u16* __restrict__ wg, const u16* __restrict__ wu,
                                                const u16* __restrict__ wd, char* __restrict__ ws, u16* __restrict__ out) {
  __shared__ __attribute__((aligned(256))) char smem[LDSB];
  LAS char* sm = (LAS char*)smem;
  const u32 lb = (u32)(size_t)sm;
  const int b = blockIdx.x, xcd = b & 7, loc = b >> 3;
  const int w = __builtin_amdgcn_readfirstlane(threadIdx.x >> 6), lane = threadIdx.x & 63;
  asm volatile("" ::"s"(x), "s"(wqkv), "s"(wphi), "s"(wo), "s"(wg), "s"(wu), "s"(wd), "s"(ws), "s"(out));
  STAMP(0);
  Ctx c;
  {
    u64 pc; u32 len, len1;
    asm volatile("s_getpc_b64 %0\n.Lfused_pc:\n\ts_mov_b32 %1, .Lfused_end0-.Lfused_pc\n\ts_mov_b32 %2, .Lfused_end1-.Lfused_pc" : "=s"(pc), "=s"(len), "=s"(len1));
    c.code = (const char*)(pc & ~15ull); c.clim = (max(len, len1) & ~15u) - 16u;
  }
  c.ti = 2 * xcd + (loc >> 4); c.jc = loc & 15;
  const int ti2 = 4 * xcd + (loc >> 3), cj = loc & 7;
  c.xt = (const char*)x + c.ti * (128 * 1024);
  c.wdc = (const char*)wd + cj * 128;
  c.wqkv = (const char*)wqkv; c.wphi = (const char*)wphi; c.wg = (const char*)wg; c.wu = (const char*)wu; c.wd = (const char*)wd; c.wo = (const char*)wo;
  c.np = c.jc < 6 ? 6 : 5;
  c.p0 = c.jc < 6 ? 6 * c.jc : 6 + 5 * c.jc;
  if (w >= 6) stage1<2, 3>(sm, lb, c, ws, w, lane);
  else if (w >= 4) stage1<1, 3>(sm, lb, c, ws, w, lane);
  else if (c.np == 6) stage1<0, 4>(sm, lb, c, ws, w, lane);
  else stage1<0, 3>(sm, lb, c, ws, w, lane);
  STAMP(14);
  vmwait<0>();
  bar();
  if (w == 0) st8d(ws + WS_F1 + (c.ti * 16 + c.jc) * 64, 0u, c.tag);
  STAMP(4);
  if (w >= 4) stage2<true>(sm, lb, c, ws, w, lane, ti2, cj, x, out);
  else stage2<false>(sm, lb, c, ws, w, lane, ti2, cj, x, out);
}

extern "C" void kernel_launch(void* const* d_in, const int* in_sizes, int n_in, void* d_out, int out_size, void* d_ws,
                              size_t ws_size, hipStream_t stream) {
  fused<<<256, 512, 0, stream>>>((const u16*)d_in[0], (const u16*)d_in[1], (const u16*)d_in[2], (const u16*)d_in[3],
                                 (const u16*)d_in[5], (const u16*)d_in[6], (const u16*)d_in[7], (char*)d_ws, (u16*)d_out);
}
```

```cpp
#include <hip/hip_runtime.h>
#include <hip/hip_bf16.h>

typedef unsigned short u16;
typedef unsigned int u32;
typedef unsigned long long u64;
typedef short s16x4 __attribute__((ext_vector_type(4)));
typedef __bf16 bf16x8 __attribute__((ext_vector_type(8)));
typedef __bf16 bf16x2 __attribute__((ext_vector_type(2)));
typedef float f32x2 __attribute__((ext_vector_type(2)));
typedef float f32x4 __attribute__((ext_vector_type(4)));
typedef float f32x16 __attribute__((ext_vector_type(16)));
typedef u32 u32x2 __attribute__((ext_vector_type(2)));
typedef u32 u32x4 __attribute__((ext_vector_type(4)));
#define LAS __attribute__((address_space(3)))
#define DI __device__ __forceinline__
#define MFMA32(a, b, c) __builtin_amdgcn_mfma_f32_32x32x16_bf16((a), (b), (c), 0, 0, 0)
#define BF8(x) __builtin_bit_cast(bf16x8, (x))

#define PRE0 0
#define PRO2 8
#define MAP1 3

constexpr int DM = 512, FF = 1376, NKS = 118;
constexpr size_t WS_H = 0;
constexpr size_t WS_KVP = WS_H + (size_t)64 * 120 * 1024;
constexpr size_t WS_KVF = WS_KVP + (size_t)16 * 32 * 8192;
constexpr size_t WS_F1 = WS_KVF + 65536;
constexpr size_t WS_F2 = WS_F1 + 16384;
constexpr size_t WS_EP = WS_F2 + 2048;
constexpr u32 SPIN_CAP = 1u << 15;

#define STAMP(s) do {} while (0)

#define DG(i) do {} while (0)
#define DG_DECL
#define DG_DUMP do {} while (0)
template <int N> struct IC { static constexpr int value = N; };
template <int B, int E, class F> DI void sfor(F&& f) { if constexpr (B < E) { f(IC<B>{}); sfor<B + 1, E>(f); } }

DI void dma16(const void* sbase, u32 voff, u32 lds) {
  asm volatile("s_mov_b32 m0, %2\n\ts_nop 0\n\tglobal_load_lds_dwordx4 %0, %1" ::"v"(voff), "s"(sbase), "s"(__builtin_amdgcn_readfirstlane(lds)) : "memory");
}
DI void dma16d(const void* sbase, u32 voff, u32 lds) {
  asm volatile("s_mov_b32 m0, %2\n\ts_nop 0\n\tglobal_load_lds_dwordx4 %0, %1 sc1" ::"v"(voff), "s"(sbase), "s"(__builtin_amdgcn_readfirstlane(lds)) : "memory");
}
template <int IMM> DI void set_m0(u32 base) { asm volatile("s_add_u32 m0, %0, %1" ::"s"(base), "n"(IMM) : "memory", "scc"); }
template <int J> DI void dma_p(u32 voff, const void* sbase) {
  asm volatile("global_load_lds_dwordx4 %0, %1 offset:%2" ::"v"(voff), "s"(sbase), "n"(J * 1024) : "memory");
}
template <int IMM, int STEP> DI void dma_ring(u32 base, u32& voff, const void* sbase) {
  asm volatile("s_add_u32 m0, %1, %3\n\tv_add_u32 %0, %4, %0\n\tglobal_load_lds_dwordx4 %0, %2 sc1" : "+v"(voff) : "s"(base), "s"(sbase), "n"(IMM), "n"(STEP) : "memory", "scc");
}
DI void dma_ring2(u32 voff, const void* sbase) { asm volatile("global_load_lds_dwordx4 %0, %1 offset:1024 sc1" ::"v"(voff), "s"(sbase) : "memory"); }
DI float vadd(float a, float b) { float d; asm("v_add_f32 %0, %1, %2" : "=v"(d) : "v"(a), "v"(b)); return d; }
DI float vsq(float q, float a) { asm("v_fmac_f32 %0, %1, %1" : "+v"(q) : "v"(a)); return q; }
DI float max3(float a, float b, float c) { float d; asm("v_max3_f32 %0, %1, %2, %3" : "=v"(d) : "v"(a), "v"(b), "v"(c)); return d; }
DI float nmin3(float a, float b, float c) { float d; asm("v_max3_f32 %0, -%1, -%2, -%3" : "=v"(d) : "v"(a), "v"(b), "v"(c)); return d; }
DI float nmin3m(float m, float b, float c) { float d; asm("v_max3_f32 %0, %1, -%2, -%3" : "=v"(d) : "v"(m), "v"(b), "v"(c)); return d; }
template <int OFF> DI void st16o(void* sbase, u32 voff, const u32x4& v) {
  asm volatile("global_store_dwordx4 %0, %1, %2 offset:%3 sc1\n\ts_nop 1" ::"v"(voff), "v"(v), "s"(sbase), "n"(OFF) : "memory");
}
DI void touch(const void* p, u32 lds) {
  asm volatile("s_mov_b32 m0, %1\n\ts_nop 0\n\tglobal_load_lds_dword %0, off" ::"v"(p), "s"(__builtin_amdgcn_readfirstlane(lds)) : "memory");
}
template <int N> DI void lgw() { asm volatile("s_waitcnt lgkmcnt(%0)" ::"n"(N) : "memory"); }
DI f32x2 pkfma(f32x2 a, f32x2 b, f32x2 c) { return __builtin_elementwise_fma(a, b, c); }
DI f32x2 pr(const f32x16& v, int i) { return (f32x2){v[i], v[i + 1]}; }
DI f32x2 sp2(float a) { return (f32x2){a, a}; }
template <int N> DI void vmwait() { asm volatile("s_waitcnt vmcnt(%0)" ::"n"(N) : "memory"); }
DI void lgwait() { asm volatile("s_waitcnt lgkmcnt(0)" ::: "memory"); }
DI void bar() { asm volatile("s_barrier" ::: "memory"); }
DI void st16d(void* sbase, u32 voff, const u32x4& v) { asm volatile("global_store_dwordx4 %0, %1, %2 sc1\n\ts_nop 1" ::"v"(voff), "v"(v), "s"(sbase) : "memory"); }
DI void st8d(void* sbase, u32 voff, const u32x2& v) { asm volatile("global_store_dwordx2 %0, %1, %2 sc1\n\ts_nop 1" ::"v"(voff), "v"(v), "s"(sbase) : "memory"); }
DI void st4d(void* sbase, u32 voff, u32 v) { asm volatile("global_store_dword %0, %1, %2 sc1\n\ts_nop 1" ::"v"(voff), "v"(v), "s"(sbase) : "memory"); }
DI void ld16(u32x4& d, const void* sbase, u32 voff) { asm volatile("global_load_dwordx4 %0, %1, %2" : "+v"(d) : "v"(voff), "s"(sbase) : "memory"); }
DI void ld16d(u32x4& d, const void* sbase, u32 voff) { asm volatile("global_load_dwordx4 %0, %1, %2 sc1" : "+v"(d) : "v"(voff), "s"(sbase) : "memory"); }
DI void ld8d(u32x2& d, const void* sbase, u32 voff) { asm volatile("global_load_dwordx2 %0, %1, %2 sc1" : "=v"(d) : "v"(voff), "s"(sbase) : "memory"); }
template <int OFF> DI void trd(u32x2& d, u32 a) { asm volatile("ds_read_b64_tr_b16 %0, %1 offset:%2" : "=v"(d) : "v"(a), "n"(OFF) : "memory"); }
template <int OFF> DI void rd128(u32x4& d, u32 a) { asm volatile("ds_read_b128 %0, %1 offset:%2" : "=v"(d) : "v"(a), "n"(OFF) : "memory"); }
DI u32x4 cat(const u32x2& a, const u32x2& b) { return (u32x4){a[0], a[1], b[0], b[1]}; }
DI void amfma(f32x16& c, const u32x4& a, const u32x4& b) { asm volatile("v_mfma_f32_32x32x16_bf16 %0, %1, %2, %0" : "+v"(c) : "v"(a), "v"(b)); }
DI void amfma16(f32x4& c, const u32x4& a, const u32x4& b) { asm volatile("v_mfma_f32_16x16x32_bf16 %0, %1, %2, %0" : "+v"(c) : "v"(a), "v"(b)); }
DI void amfma0(f32x16& c, const u32x4& a, const u32x4& b) { asm volatile("v_mfma_f32_32x32x16_bf16 %0, %1, %2, 0" : "=&v"(c) : "v"(a), "v"(b)); }
DI void amfma_n(f32x16& c, const u32x4& a, const u32x4& b) { asm volatile("s_nop 1\n\tv_mfma_f32_32x32x16_bf16 %0, %1, %2, %0" : "+v"(c) : "v"(a), "v"(b)); }

DI bf16x8 tr_frag(LAS char* p0, LAS char* p1) {
  s16x4 a = __builtin_amdgcn_ds_read_tr16_b64_v4i16((LAS s16x4*)p0);
  s16x4 b = __builtin_amdgcn_ds_read_tr16_b64_v4i16((LAS s16x4*)p1);
  return BF8(__builtin_shufflevector(a, b, 0, 1, 2, 3, 4, 5, 6, 7));
}
DI u32 pk2(float a, float b) { f32x2 v = {a, b}; return __builtin_bit_cast(u32, __builtin_convertvector(v, bf16x2)); }
DI u32 pkv(f32x2 v) { return __builtin_bit_cast(u32, __builtin_convertvector(v, bf16x2)); }
DI float bfr(float a) { return __uint_as_float(pk2(a, a) & 0xffff0000u); }
DI float lo_f(u32 w) { return __uint_as_float(w << 16); }
DI float hi_f(u32 w) { return __uint_as_float(w & 0xffff0000u); }
DI float fexp2(float x) { return __builtin_amdgcn_exp2f(x); }
DI float frcp(float x) { return __builtin_amdgcn_rcpf(x); }
DI void swap32(float& a, float& b) {
  auto r = __builtin_amdgcn_permlane32_swap(__float_as_uint(a), __float_as_uint(b), false, false);
  a = __uint_as_float(r[0]); b = __uint_as_float(r[1]);
}
template <int C> DI float dpp_add(float a) { return a + __uint_as_float(__builtin_amdgcn_update_dpp(0, __float_as_uint(a), C, 0xf, 0xf, true)); }
DI float wave_sum(float a) {
  a = dpp_add<0xB1>(a); a = dpp_add<0x4E>(a); a = dpp_add<0x141>(a); a = dpp_add<0x140>(a);
  auto r = __builtin_amdgcn_permlane16_swap(__float_as_uint(a), __float_as_uint(a), false, false);
  a = __uint_as_float(r[0]) + __uint_as_float(r[1]);
  float b = a; swap32(a, b);
  return a + b;
}
DI void poll8(const void* sbase, u32 voff, u32x2 tag) {
#pragma unroll 1
  for (u32 spin = 0; spin < SPIN_CAP; spin++) {
    u32x2 v;
    asm volatile("global_load_dwordx2 %0, %1, %2 sc1\n\ts_waitcnt vmcnt(0)" : "=&v"(v) : "v"(voff), "s"(sbase) : "memory");
    if (__all(v[0] == tag[0] && v[1] == tag[1])) break;
    __builtin_amdgcn_s_sleep(2);
  }
}

DI void ld8_w0(u32x2& a, const void* sbase, u32 voff, int w) {
  asm volatile("s_cmp_lg_u32 %3, 0\n\ts_cbranch_scc1 .Lw0%=\n\tglobal_load_dwordx2 %0, %1, %2 sc1\n.Lw0%=:" : "=&v"(a) : "v"(voff), "s"(sbase), "s"(w) : "memory", "scc");
}
DI void poll_all(const void* sbase, u32 voff, u32x2 tag) {
#pragma unroll 1
  for (u32 spin = 0; spin < SPIN_CAP; spin++) {
    u32x2 a, b, c, d;
    asm volatile("global_load_dwordx2 %0, %4, %6 sc1\n\tglobal_load_dwordx2 %1, %4, %7 sc1\n\tglobal_load_dwordx2 %2, %5, %6 sc1\n\tglobal_load_dwordx2 %3, %5, %7 sc1\n\ts_waitcnt vmcnt(0)"
                 : "=&v"(a), "=&v"(b), "=&v"(c), "=&v"(d) : "v"(voff), "v"(voff + 8192), "s"(sbase), "s"((const char*)sbase + 4096) : "memory");
    const u32 x = ((a[0] ^ tag[0]) | (a[1] ^ tag[1]) | (b[0] ^ tag[0])) | ((b[1] ^ tag[1]) | (c[0] ^ tag[0]) | (c[1] ^ tag[1])) | ((d[0] ^ tag[0]) | (d[1] ^ tag[1]));
    if (__all(x == 0)) break;
    __builtin_amdgcn_s_sleep(2);
  }
}

constexpr int UPO = 6176, QO = 12352, WS1 = 14464, XR = 7 * WS1, XSLOT = 16384;
constexpr int P0R = 5 * WS1;
constexpr int VIMG = XR + 3 * XSLOT, STAT = VIMG + 8192, WPHI = STAT + 1024, JUNK = WPHI + 1024, RST = JUNK + 1280;
constexpr int SLOT2 = 12288, WO_L = 8 * SLOT2, LDSB = 163840;
constexpr int R1F = 38;
DI constexpr int piece_l(int n) {
  return n < 84 ? ((n / 14 + 2) % 7) * WS1 + (n % 14 < 6 ? (n % 14) * 1024 : n % 14 < 12 ? UPO + (n % 14 - 6) * 1024 : QO + (n % 14 - 12) * 1024)
       : n < 116 ? XR + XSLOT + (n - 84) * 1024 : n < 128 ? WS1 + (n - 116) * 1024 : n < 144 ? XR + (n - 128) * 1024 : VIMG + (n - 144) * 1024;
}
DI int grp_l(int G) { return G < 3 ? WS1 + G * 4096 : G < 7 ? XR + (G - 3) * 4096 : VIMG + (G - 7) * 4096; }
DI void dma_grp(const char* src, u32 lds, const u32 (&vq)[4]) {
  asm volatile("s_mov_b32 m0, %0\n\ts_nop 0" ::"s"(__builtin_amdgcn_readfirstlane(lds)) : "memory");
  dma_p<0>(vq[0], src); dma_p<1>(vq[1], src); dma_p<2>(vq[2], src); dma_p<3>(vq[3], src);
}
struct Ctx {
  const char *xt, *wdc;
  const char *x, *wqkv, *wphi, *wg, *wu, *wd, *wo;
  int np, ti, jc, p0;
  u32x2 tag;
  const char* code; u32 clim;
};
DI u32 two_bf(float v) { const float hi = bfr(v); return pk2(hi, v - hi); }

template <bool NORM>
DI void dual_softmax(const f32x16& p, float sc, u32 (&fp)[8], u32 (&fm)[8]) {
  const float mx = max3(max3(max3(p[0], p[1], p[2]), max3(p[3], p[4], p[5]), max3(p[6], p[7], p[8])),
                        max3(max3(p[9], p[10], p[11]), p[12], p[13]), max3(p[14], p[15], p[15])) * -sc;
  const float nn = max3(max3(nmin3(p[0], p[1], p[2]), nmin3(p[3], p[4], p[5]), nmin3(p[6], p[7], p[8])),
                        nmin3m(nmin3(p[9], p[10], p[11]), p[12], p[13]), nmin3(p[14], p[15], p[15])) * -sc;
  f32x2 ep[8], em[8];
#pragma unroll
  for (int j = 0; j < 8; j++) { ep[j] = pkfma(pr(p, 2 * j), sp2(sc), sp2(mx)); em[j] = pkfma(pr(p, 2 * j), sp2(-sc), sp2(nn)); }
#pragma unroll
  for (int j = 0; j < 8; j++) {
    ep[j] = (f32x2){fexp2(ep[j][0]), fexp2(ep[j][1])};
    em[j] = (f32x2){fexp2(em[j][0]), fexp2(em[j][1])};
  }
  const f32x2 tp = ((ep[0] + ep[1]) + (ep[2] + ep[3])) + ((ep[4] + ep[5]) + (ep[6] + ep[7]));
  const f32x2 tm = ((em[0] + em[1]) + (em[2] + em[3])) + ((em[4] + em[5]) + (em[6] + em[7]));
  float sp = tp[0] + tp[1], sm = frcp(tm[0] + tm[1]);
  if (NORM) sp = frcp(sp); else sm *= sp;
#pragma unroll
  for (int j = 0; j < 8; j++) {
    if (NORM) ep[j] *= sp2(sp);
    fp[j] = pkv(ep[j]); fm[j] = pkv(em[j] * sp2(sm));
  }
}

template <int ROLE, int NT>
DI void stage1(LAS char* sm, u32 lb, Ctx& c, char* ws, int w, int lane) {
  constexpr bool PG = ROLE != 0;
  const int tg = w & 3;
  const int r = lane & 31, h = lane >> 5, g16 = (lane >> 4) & 1, q4 = (lane & 15) >> 2, pp = lane & 3;
  const u32 vlin = lane * 16;
  u32 voff[4];
  const char *src, *cur = nullptr;
  u32 lds, sstep = 0;
  if constexpr (ROLE == 2) {
    const int rr = lane >> 3, pos = lane & 7, f = lane >> 4;
    voff[0] = rr * 1024 + 16 * (pos ^ f);
    voff[1] = rr * 1024 + 16 * (pos ^ f ^ 4) + 7168;
    voff[2] = voff[0] + 14336; voff[3] = voff[1] + 14336;
    src = c.xt + (w - 6) * 65536;
    lds = lb + XR + (w - 6) * 8192;
  } else if constexpr (ROLE == 1) {
#pragma unroll
    for (int j = 0; j < 4; j++) voff[j] = (lane >> 2) * 3072 + (lane & 3) * 16 + j * (16 * 3072 - 1024);
    src = c.wqkv + (w - 3) * 1024 + c.jc * 64;
    lds = lb + P0R + (w - 4) * 4096;
  }
  u64 ep;
  asm volatile("s_load_dwordx2 %0, %1, 0x0" : "=s"(ep) : "s"(ws + WS_EP) : "memory");
  auto quad = [&] { dma_p<0>(voff[0], cur); dma_p<1>(voff[1], cur); dma_p<2>(voff[2], cur); dma_p<3>(voff[3], cur); };
  auto xset = [&](auto sl_, auto hf_) {
    constexpr int sl = decltype(sl_)::value, hf = decltype(hf_)::value;
    set_m0<(sl % 3) * XSLOT + hf * 4096>(lds);
    cur = src + ((sl & 7) * 128 + hf * 32768);
  };
  auto kvset = [&](auto s_) { constexpr int s = decltype(s_)::value; set_m0<(s % 3) * 8192>(lds); cur = src + s * (64 * 3072); };
  auto lines = [&](int which) {
    const int b = blockIdx.x;
    const char* p;
    if (which <= 0) {
      const int g = b * 36 + min(lane, 35), s0 = which ? 5 : 2;
      if (g < 6144) p = c.x + (g / 3) * 1024 + (s0 + g % 3) * 128;
      else p = c.wqkv + (s0 * 64 + ((g - 6144) >> 4)) * 3072 + 1024 + (g & 15) * 128;
    } else if (which == 4) p = c.wqkv + (b * 2 + (lane >> 3 & 1)) * 3072 + (lane & 7) * 128;
    else if (which < 3) { const int g = b * 43 + min(lane, 42); p = (g < 5504 ? c.wg + g * 128 : c.wu + (g - 5504) * 128) + (which - 1) * 704512; }
    else { const int g = b * 59 + min(lane, 58); p = g < 11008 ? c.wd + g * 128 : c.wo + (g - 11008) * 128; }
    touch(p, lb + JUNK + 1024);
  };
  auto code = [&](int half) { dma16(c.code, min((blockIdx.x >> 3) * 1024 + vlin + half * 32768, c.clim), lb + JUNK); };
  if constexpr (ROLE == 2) {
    lds = __builtin_amdgcn_readfirstlane(lds);
    xset(IC<1>{}, IC<0>{}); asm volatile("s_nop 0"); quad(); xset(IC<1>{}, IC<1>{}); asm volatile("s_nop 0"); quad();
  } else if constexpr (ROLE == 1) {
    lds = __builtin_amdgcn_readfirstlane(lds);
    kvset(IC<1>{}); asm volatile("s_nop 0"); quad();
  } else {
    if (w == 0) dma16(c.wphi + c.jc * 1024, vlin, lb + WPHI);
    if (w == 2) lines(0);
    const int emax = 2 * c.np - 1;
#pragma unroll
    for (int m = 0; m < 3; m++) {
      const int ch = 64 * (3 * (w & 1) + m) + lane, row = (ch * 5462) >> 16;
      int e = ch - row * 12; e = e > emax ? emax : e;
      voff[m] = (u32)(row * (2 * FF) + 16 * e) - m * 1024;
    }
    voff[3] = 0;
    src = ((w >> 1) ? c.wu : c.wg) + c.p0 * 32;
    lds = __builtin_amdgcn_readfirstlane(lb + (w >> 1) * UPO + (w & 1) * 3072);
    sstep = 64 * FF;
  }
  const u32 vw = (lane >> 3) * 1024 + 16 * ((lane & 7) ^ (((lane >> 4) & 1) << 2));
  u32 xa[4], ka = lb + P0R + (PG ? 0 : 4096) + (8 * h + q4) * 64 + 32 * g16 + 8 * pp;
#pragma unroll
  for (int k = 0; k < 4; k++) { xa[k] = (lb + XR + (32 * tg + r) * 128 + 16 * (h ^ ((r >> 1) & 7))) ^ (32 * k); asm volatile("" : "+v"(xa[k])); }
  f32x16 acc[NT], zero;
#pragma unroll
  for (int j = 0; j < 16; j++) zero[j] = 0.f;
  acc[0] = zero;
  f32x4 cs0 = {0.f, 0.f, 0.f, 0.f}, cs1 = cs0;
  const u32 one = ((lane & 31) == 0 || (lane & 31) == 20) ? 0x3f803f80u : 0u;
  u32x4 sel = {one, one, one, one};
  asm volatile("s_waitcnt lgkmcnt(0)" : "+s"(ep), "+v"(cs0), "+v"(cs1), "+v"(sel), "+v"(ka), "+v"(acc[0]) :: "memory");
  ep += 1;
  c.tag = (u32x2){(u32)ep, (u32)(ep >> 32)};
  u32x2 A[2][NT][2], A0[4][2];
  u32x4 B[2], B0[4];
  f32x2 st1[2] = {{0.f, 0.f}, {0.f, 0.f}}, st2[2] = {{0.f, 0.f}, {0.f, 0.f}};
  float piv = 0.f;
  auto rdx = [&](auto k_) { constexpr int k = decltype(k_)::value; rd128<((k >> 2) % 3) * XSLOT>(B[k & 1], xa[k & 3]); };
  auto rdk = [&](auto k_) {
    constexpr int k = decltype(k_)::value, O = ((k >> 2) % 3) * 8192 + (k & 3) * 1024;
    rd128<((k >> 2) % 3) * XSLOT>(B0[k & 3], xa[k & 3]);
    trd<O>(A0[k & 3][0], ka); trd<O + 256>(A0[k & 3][1], ka);
  };
  auto wset = [&](auto u_) {
    constexpr int U = decltype(u_)::value;
    if constexpr (U == 16) {
      src = c.wdc + (ROLE == 0 ? 3 * w : 8 + w) * 8192; sstep = 14 * 8192;
      voff[0] = vw; voff[1] = vw + 7168; voff[2] = vw + 14336;
    }
    set_m0<(U % 7) * WS1>(lds); cur = src; src += sstep;
  };
  auto p0_pieces = [&](auto k_) {
    constexpr int k = decltype(k_)::value, sl = ((k + 2) >> 2) + 2, j = (k + 2) & 3;
    if constexpr (ROLE == 2) {
      if constexpr (k <= 29) {
        if constexpr (!(j & 1)) { xset(IC<sl>{}, IC<(j >> 1)>{}); asm volatile("s_nop 0"); }
        dma_p<2 * (j & 1)>(voff[2 * (j & 1)], cur); dma_p<2 * (j & 1) + 1>(voff[2 * (j & 1) + 1], cur);
      }
    } else if constexpr (ROLE == 1) {
      if constexpr (k <= 21) {
        if constexpr (j == 0) { kvset(IC<sl>{}); asm volatile("s_nop 0"); }
        dma_p<j>(voff[j], cur);
      } else if constexpr (k >= 27) {
        if constexpr (k == 27) {
          src = c.wqkv + c.jc * 64 + (w - 4) * (16 * 3072); sstep = 32 * 3072;
          lds = __builtin_amdgcn_readfirstlane(lb + QO + (w - 4) * 1024);
        }
        wset(IC<k - 27>{}); asm volatile("s_nop 0"); dma_p<0>(voff[0], cur);
      }
    }
  };
  if constexpr (ROLE == 0) { if (w == 2) vmwait<1>(); else vmwait<0>(); } else vmwait<(ROLE == 2 ? 8 : 4)>();
  bar();
  STAMP(1);
  rdk(IC<0>{}); rdk(IC<1>{});
  p0_pieces(IC<-2>{}); p0_pieces(IC<-1>{});
  sfor<0, 32>([&](auto k_) {
    constexpr int k = decltype(k_)::value, C = k & 3;
    constexpr bool SY = (k & 3) == 2 && k < 30;
    if constexpr (SY || k == 31) lgwait(); else lgw<3>();
    if constexpr (ROLE == 0) asm volatile("" : "+v"(B0[C]));
    amfma(acc[0], cat(A0[C][0], A0[C][1]), B0[C]);
    if constexpr (SY) {
      if constexpr (ROLE == 2) vmwait<8>(); else if constexpr (ROLE == 1) vmwait<(k == 26 ? 0 : 4)>();
      bar();
    }
    if constexpr (k < 30) rdk(IC<k + 2>{});
    if constexpr (ROLE == 0 && (k == 1 || k == 7 || k == 13 || k == 19)) { if (w == (k == 1 ? 3 : k == 7 ? 0 : k == 13 ? 1 : 2)) lines(k == 1 ? -1 : k == 7 ? 1 : k == 13 ? 4 : 2); }
    p0_pieces(k_);
    amfma16(cs0, sel, cat(A0[C][0], A0[C][1]));
    if constexpr (ROLE == 0) {
#pragma unroll
      for (int j = 0; j < 4; j++) {
        const float a = lo_f(B0[C][j]), b = hi_f(B0[C][j]);
        st1[0][0] = vadd(st1[0][0], a); st1[0][1] = vadd(st1[0][1], b);
        st2[0][0] = vsq(st2[0][0], a); st2[0][1] = vsq(st2[0][1], b);
      }
      asm volatile("" : "+v"(st1[0]), "+v"(st1[1]), "+v"(st2[0]), "+v"(st2[1]));
    }
  });
  f32x2 rst;
  if constexpr (ROLE == 0) {
    const f32x2 a = st1[0] + st1[1], q = st2[0] + st2[1];
    float s1 = a[0] + a[1], s2 = q[0] + q[1], t1 = s1, t2 = s2;
    swap32(s1, t1); swap32(s2, t2);
    const float ms = (s1 + t1) * (1.f / 512.f), mq = (s2 + t2) * (1.f / 512.f);
    rst = (f32x2){bfr(__builtin_amdgcn_sqrtf(bfr(bfr(fmaxf(mq - ms * ms, 0.f)) + 1e-5f))), bfr(piv + ms)};
    *(LAS f32x2*)(sm + RST + (32 * tg + r) * 8) = rst;
    lgwait();
  }
  asm volatile("s_nop 15\n\ts_barrier" : "+v"(cs0), "+v"(acc[0]) :: "memory");
  STAMP(2);
  if constexpr (PG) rst = *(LAS f32x2*)(sm + RST + (32 * tg + r) * 8);
  const float sd = rst[0], rs = frcp(sd), sc = rs * 1.4426950408889634f;
  const u32x4 mfrag = {h ? 0u : ((__float_as_uint(rst[1]) >> 16) ^ 0x8000u) * 0x10001u, 0u, 0u, 0u};
  const bool b2 = (r >> 2) & 1;
  auto phi = [&](const f32x16& a) {
    LAS char* wp = sm + WPHI + (pp & 1) * 512 + (4 * h + q4) * 32 + 8 * (2 * g16 + (pp >> 1));
    const u32x4 wv = __builtin_bit_cast(u32x4, tr_frag(wp, wp + 256));
    u32x4 wA, wB, qa, qb;
#pragma unroll
    for (int j = 0; j < 4; j++) {
      wA[j] = b2 ? 0u : wv[j]; wB[j] = b2 ? wv[j] : 0u;
      qa[j] = pkv(pr(a, 2 * j)); qb[j] = pkv(pr(a, 8 + 2 * j));
    }
    f32x16 P = MFMA32(BF8(wB), BF8(qb), MFMA32(BF8(wA), BF8(qa), zero));
    asm volatile("s_nop 15" : "+v"(P));
    return P;
  };
  acc[0] = MFMA32(BF8(((u32x4){two_bf(cs0[0]), 0u, 0u, 0u})), BF8(mfrag), acc[0]);
  {
    const int tok = 32 * tg + r;
    if constexpr (PG) {
      u32 fp[8], fm[8];
      dual_softmax<true>(phi(acc[0]), sc, fp, fm);
      LAS char* pd = sm + P0R + h * 16384 + tok * 64;
      const int fsw = (r >> 1) & 7;
#pragma unroll
      for (int k = 0; k < 4; k++) {
        const int u = 2 * (k & 1) + (k >> 1);
        *(LAS u32x2*)(pd + 8 * (u ^ fsw)) = (u32x2){fp[2 * k], fp[2 * k + 1]};
        *(LAS u32x2*)(pd + 8 * ((4 + u) ^ fsw)) = (u32x2){fm[2 * k], fm[2 * k + 1]};
      }
    } else {
      LAS char* vd = sm + VIMG + tok * 32;
      const int vsw = (r >> 2) & 3;
#pragma unroll
      for (int e = 0; e < 2; e++) {
        *(LAS u32x2*)(vd + e * 4096 + 8 * ((0 + h) ^ vsw)) = (u32x2){pkv(pr(acc[0], 8 * e) * sp2(rs)), pkv(pr(acc[0], 8 * e + 2) * sp2(rs))};
        *(LAS u32x2*)(vd + e * 4096 + 8 * ((2 + h) ^ vsw)) = (u32x2){pkv(pr(acc[0], 8 * e + 4) * sp2(rs)), pkv(pr(acc[0], 8 * e + 6) * sp2(rs))};
      }
      sfor<PRE0, 5>([&](auto u_) { wset(u_); asm volatile("s_nop 0"); dma_p<0>(voff[0], cur); dma_p<1>(voff[1], cur); dma_p<2>(voff[2], cur); });
    }
  }
  u32 wl[NT], wh[NT];
#pragma unroll
  for (int s = 0; s < NT; s++) {
    int t = s + tg;
    if constexpr (PG) t = c.np - 2 + (t & 1); else t = t >= NT ? t - NT : t;
    wl[s] = (PG && s == 2) ? lb + QO + (8 * h + q4) * 64 + 32 * g16 + 8 * pp : lb + g16 * UPO + (8 * h + q4) * 192 + 8 * pp + 32 * t;
    wh[s] = wl[s] + 4 * WS1;
    acc[s] = zero;
    asm volatile("" : "+v"(acc[s]), "+v"(wl[s]), "+v"(wh[s]));
  }
  cs0 = (f32x4){0.f, 0.f, 0.f, 0.f};
  asm volatile("" : "+v"(cs0));
  lgwait();
  bar();
  STAMP(12);
  if constexpr (ROLE == 2) {
    xset(IC<10>{}, IC<0>{}); asm volatile("s_nop 0"); quad(); xset(IC<10>{}, IC<1>{}); asm volatile("s_nop 0"); quad();
  }
  if constexpr (ROLE == 1) {
    const int t0 = 8 * h + q4;
    const u32 pb = lb + P0R + (w - 4) * 16384, vb = lb + VIMG + (w - 4) * 4096;
    const u32 pa0 = pb + t0 * 64 + 8 * ((4 * g16 + pp) ^ (t0 >> 1)), pa1 = pb + (t0 + 4) * 64 + 8 * ((4 * g16 + pp) ^ ((t0 >> 1) + 2));
    const u32 pb0 = vb + t0 * 32 + 8 * (pp ^ (2 * h)), pb1 = vb + (t0 + 4) * 32 + 8 * (pp ^ (2 * h + 1));
    u32x2 fa[8][2], fb[8][2];
    sfor<0, 8>([&](auto k_) {
      constexpr int k = decltype(k_)::value;
      trd<k * 1024>(fa[k][0], pa0); trd<k * 1024>(fa[k][1], pa1); trd<k * 512>(fb[k][0], pb0); trd<k * 512>(fb[k][1], pb1);
    });
    f32x16 kv = zero;
    sfor<0, 8>([&](auto k_) {
      constexpr int k = decltype(k_)::value;
      if constexpr (k == 0) lgw<15>();
      if constexpr (k == 4) lgwait();
      asm volatile("" : "+v"(fa[k][0]), "+v"(fa[k][1]), "+v"(fb[k][0]), "+v"(fb[k][1]));
      u32x4 bq = cat(fb[k][0], fb[k][1]);
#pragma unroll
      for (int j = 0; j < 4; j++) bq[j] = g16 ? 0x3f803f80u : bq[j];
      kv = MFMA32(BF8(cat(fa[k][0], fa[k][1])), BF8(bq), kv);
    });
    char* dst = ws + WS_KVP + (size_t)(c.ti * 32 + 2 * c.jc + w - 4) * 8192;
    const u32 vo = (r > 16 ? (lane & 32) + 16 : lane) * 16;
    sfor<0, 8>([&](auto p_) {
      constexpr int p = decltype(p_)::value;
      st16o<(p & 3) * 1024>(dst + (p >> 2) * 4096, vo, (u32x4){__float_as_uint(kv[2 * p]), __float_as_uint(kv[2 * p + 1]), c.tag[0], c.tag[1]});
    });
  }
  auto rdw = [&](auto k_, auto s_) {
    constexpr int k = decltype(k_)::value, s = decltype(s_)::value, sl = (k >> 1) % 7, Q = PG && s == 2;
    constexpr int O = (sl & 3) * WS1 + (k & 1) * (Q ? 1024 : 3072);
    trd<O>(A[k & 1][s][0], sl < 4 ? wl[s] : wh[s]); trd<O + (Q ? 256 : 768)>(A[k & 1][s][1], sl < 4 ? wl[s] : wh[s]);
  };
  auto p1_set = [&](auto v_) {
    constexpr int v = decltype(v_)::value;
    if constexpr (ROLE == 2) {
      if constexpr (v == 12 || v == 14) {
        src = c.wdc + ((v == 12 ? 84 : 100) + (w - 6) * 8) * 8192;
#pragma unroll
        for (int j = 0; j < 4; j++) voff[j] = vw + j * 7168;
      }
      if constexpr (v >= 12) { set_m0<((10 + (v >> 1)) % 3) * XSLOT + (v & 1) * 4096>(lds); cur = src + (v & 1) * 32768; }
      else if constexpr (v >= 2) xset(IC<10 + (v >> 1)>{}, IC<(v & 1)>{});
    } else wset(IC<v + 6>{});
  };
  auto p1_piece = [&](auto v_, auto j_) {
    constexpr int v = decltype(v_)::value, j = decltype(j_)::value;
    if constexpr (v <= 15 && (ROLE == 2 ? v >= 2 : j < (ROLE == 0 ? 3 : 1))) dma_p<j>(voff[j], cur);
  };
  vmwait<(ROLE == 2 ? 16 : 12)>();
  bar();
  STAMP(13);
  rdx(IC<32>{});
  sfor<0, NT>([&](auto s_) { rdw(IC<0>{}, s_); });
  if constexpr (ROLE != 2) {
    wset(IC<5>{}); asm volatile("s_nop 0"); sfor<0, 3>([&](auto j_) { p1_piece(IC<0>{}, j_); });
    wset(IC<6>{}); asm volatile("s_nop 0"); p1_piece(IC<0>{}, IC<0>{}); p1_piece(IC<0>{}, IC<1>{});
  }
  u32x4 part[16], kvf[4];
  u32x2 f2;
  const int khead = 2 * c.jc + (c.ti >> 3), kp = c.ti & 7;
  const char* psrc = ws + WS_KVP + khead * 8192 + kp * 1024;
  const u32 pvo = (r > 16 ? (lane & 32) + 16 : lane) * 16;
  sfor<0, 32>([&](auto k_) {
    constexpr int k = decltype(k_)::value, C = k & 1, v = (k + 1) >> 1;
    constexpr bool N = k < 31;
    using K1 = IC<k + 1>;
    lgwait();
    amfma(acc[0], cat(A[C][0][0], A[C][0][1]), B[C]);
    if constexpr (C && v < 16) {
      if constexpr (ROLE == 0) vmwait<15>();
      else if constexpr (ROLE == 1) vmwait<(v <= 4 ? 13 : v == 5 ? 13 : v <= 7 ? 21 : 5)>();
      else if constexpr (!(v & 1)) vmwait<8>();
      bar();
      if constexpr (ROLE == 0 && v == 3) { if (w == 0) lines(3); }
      if constexpr (ROLE == 0 && v == 9) { if (w >= 2) code(w & 1); }
      if constexpr (ROLE == 1 && v == 7) {
        vmwait<1>();
        u32 bad = 0;
#pragma unroll
        for (int i = 0; i < 16; i++) { asm volatile("" : "+v"(part[i])); bad |= (part[i][2] ^ c.tag[0]) | (part[i][3] ^ c.tag[1]); }
#pragma unroll 1
        for (u32 spin = 0; spin < SPIN_CAP && !__all(bad == 0); spin++) {
          __builtin_amdgcn_s_sleep(2);
#pragma unroll
          for (int i = 0; i < 16; i++) ld16d(part[i], psrc + (size_t)i * 32 * 8192, pvo);
          vmwait<0>();
          bad = 0;
#pragma unroll
          for (int i = 0; i < 16; i++) { asm volatile("" : "+v"(part[i])); bad |= (part[i][2] ^ c.tag[0]) | (part[i][3] ^ c.tag[1]); }
        }
        f32x2 s2 = {0.f, 0.f};
#pragma unroll
        for (int i = 0; i < 16; i++) s2 += (f32x2){__uint_as_float(part[i][0]), __uint_as_float(part[i][1])};
        if (r >= 16 && kp < 4) s2 += sp2(1e-6f);
        st4d(ws + WS_KVF + khead * 2048 + (kp >> 2) * 1024 + (kp & 3) * 4, vlin, pkv(s2));
      }
      if constexpr (ROLE == 1 && v == 9) { vmwait<2>(); st8d(ws + WS_F2 + c.jc * 128 + c.ti * 8, 0u, c.tag); }
      if constexpr (PG && v == 13) ld8d(f2, ws + WS_F2 + c.jc * 128, (lane & 15) * 8);
      if constexpr (PG && v == 15) {
        vmwait<(ROLE == 1 ? 2 : 8)>();
        asm volatile("" : "+v"(f2));
        if (!__all(f2[0] == c.tag[0] && f2[1] == c.tag[1])) poll8(ws + WS_F2 + c.jc * 128, (lane & 15) * 8, c.tag);
        const char* ks = ws + WS_KVF + c.jc * 4096;
        u32x4 t0, t1, t2, t3;
        asm volatile("global_load_dwordx4 %0, %4, %5 sc1\n\tglobal_load_dwordx4 %1, %4, %5 offset:1024 sc1\n\tglobal_load_dwordx4 %2, %4, %5 offset:2048 sc1\n\tglobal_load_dwordx4 %3, %4, %5 offset:3072 sc1"
                     : "=&v"(t0), "=&v"(t1), "=&v"(t2), "=&v"(t3) : "v"(vlin), "s"(ks) : "memory");
        kvf[0] = t0; kvf[1] = t1; kvf[2] = t2; kvf[3] = t3;
      }
      p1_set(IC<v>{});
    }
    if constexpr (N) { rdx(IC<k + 33>{}); rdw(K1{}, IC<0>{}); }
    p1_piece(IC<v>{}, IC<(C ? 0 : 2)>{});
    if constexpr (ROLE == 1 && k >= 7 && k <= 10) {
#pragma unroll
      for (int i = 4 * (k - 7); i < 4 * (k - 6); i++) { u32x4 t; asm volatile("global_load_dwordx4 %0, %1, %2 sc1" : "=v"(t) : "v"(pvo), "s"(psrc + (size_t)i * 32 * 8192) : "memory"); part[i] = t; }
    }
    amfma(acc[1], cat(A[C][1][0], A[C][1][1]), B[C]);
    if constexpr (N) { rdw(K1{}, IC<1>{}); rdw(K1{}, IC<2>{}); }
    amfma16(cs0, sel, cat(A[C][0][0], A[C][0][1]));
    amfma(acc[2], cat(A[C][2][0], A[C][2][1]), B[C]);
    if constexpr (N && NT == 4) rdw(K1{}, IC<NT - 1>{});
    p1_piece(IC<v>{}, IC<(C ? 1 : 3)>{});
    if constexpr (PG) amfma16(cs1, sel, cat(A[C][2][0], A[C][2][1]));
    if constexpr (NT == 4) amfma(acc[NT - 1], cat(A[C][NT - 1][0], A[C][NT - 1][1]), B[C]);
  });
  asm volatile("s_nop 15" : "+v"(cs0), "+v"(cs1) :: "memory");
#pragma unroll
  for (int s = 0; s < NT; s++) asm volatile("" : "+v"(acc[s]));
  STAMP(3);
  DG_DECL;
  DG(0);
  auto tile = [&](int s) { int t = s + tg; if constexpr (PG) t = c.np - 2 + (t & 1); else t = t >= NT ? t - NT : t; return t; };
  if (lane < 32) *(LAS u32*)(sm + STAT + (tile(0) * 32 + lane) * 4) = two_bf(cs0[0]);
  u32 vq[4];
#pragma unroll
  for (int j = 0; j < 4; j++) vq[j] = vw + j * 7168;
  f32x16 Pq;
  if constexpr (PG) Pq = phi(MFMA32(BF8(((u32x4){two_bf(cs1[0]), 0u, 0u, 0u})), BF8(mfrag), acc[2]));
  DG(1);
  lgwait();
  bar();
  DG(2);
  constexpr int NF = PG ? 2 : NT;
  u32 cf[NF];
#pragma unroll
  for (int s = 0; s < NF; s++) cf[s] = *(LAS u32*)(sm + STAT + (tile(s) * 32 + r) * 4);
  dma_grp(c.wdc + (116 + 4 * w) * 8192, lb + grp_l(w), vq);
  if (ROLE == 1 && w == 4) dma_grp(c.wdc + 148 * 8192, lb + grp_l(8), vq);
#pragma unroll
  for (int s = 0; s < NF; s++) acc[s] = MFMA32(BF8(((u32x4){cf[s], 0u, 0u, 0u})), BF8(mfrag), acc[s]);
  char* dst = ws + WS_H + (size_t)(4 * c.ti + tg) * (NKS * 1024);
  {
    const float K0 = -2.f * 0.7978845608028654f * 1.4426950408889634f * rs, K1 = K0 * 0.044715f * rs * rs, v2 = sd * sd;
#pragma unroll
    for (int s = 0; s < NF; s++) {
      DG(3 + s);
      f32x2 e[4], gu[4];
#pragma unroll
      for (int j = 0; j < 4; j++) { const f32x2 g = pr(acc[s], 2 * j); e[j] = g * pkfma(g * g, sp2(K1), sp2(K0)); gu[j] = g * pr(acc[s], 8 + 2 * j); }
#pragma unroll
      for (int j = 0; j < 4; j++) e[j] = pkfma((f32x2){fexp2(e[j][0]), fexp2(e[j][1])}, sp2(v2), sp2(v2));
#pragma unroll
      for (int j = 0; j < 4; j++) gu[j] *= (f32x2){frcp(e[j][0]), frcp(e[j][1])};
      st16d(dst + c.p0 * 1024, vlin + tile(s) * 1024, (u32x4){pkv(gu[0]), pkv(gu[1]), pkv(gu[2]), pkv(gu[3])});
    }
  }
  DG(7);
  u32 fp[8], fm[8];
  if constexpr (PG) {
    asm volatile("" : "+v"(Pq));
    dual_softmax<false>(Pq, sc, fp, fm);
#pragma unroll
    for (int i = 0; i < 4; i++) {
      auto a = __builtin_amdgcn_permlane32_swap(fp[i], fp[i + 4], false, false); fp[i] = a[0]; fp[i + 4] = a[1];
      auto b = __builtin_amdgcn_permlane32_swap(fm[i], fm[i + 4], false, false); fm[i] = b[0]; fm[i + 4] = b[1];
    }
#pragma unroll
    for (int i = 0; i < 8; i++) asm volatile("" : "+v"(fp[i]), "+v"(fm[i]));
  }
  STAMP(14);
  DG(8);
  vmwait<0>();
  DG(9);
  bar();
  DG(10);
  if (ROLE == 0 && w == 0) st8d(ws + WS_F1 + (c.ti * 16 + c.jc) * 64, 0u, c.tag);
  STAMP(4);
  if constexpr (PG) {
    asm volatile("" : "+v"(kvf[0]), "+v"(kvf[1]), "+v"(kvf[2]), "+v"(kvf[3]));
#pragma unroll
    for (int e = 0; e < 2; e++) {
      f32x16 n = MFMA32(BF8(kvf[2 * e]), BF8(((u32x4){fp[4 * e], fp[4 * e + 1], fp[4 * e + 2], fp[4 * e + 3]})), zero);
      n = MFMA32(BF8(kvf[2 * e + 1]), BF8(((u32x4){fm[4 * e], fm[4 * e + 1], fm[4 * e + 2], fm[4 * e + 3]})), n);
      const f32x2 rd = sp2(frcp(n[8]));
      st16d(dst + (86 + 2 * c.jc + e) * 1024, vlin, (u32x4){pkv(pr(n, 0) * rd), pkv(pr(n, 2) * rd), pkv(pr(n, 4) * rd), pkv(pr(n, 6) * rd)});
    }
  }
  DG(11);
  DG_DUMP;
}

constexpr int need_n(bool lo, int t) {
  const int n = lo ? 2 : 1, last = t <= 2 ? 7 : (t + 5 > 19 ? 19 : t + 5);
  return (last - t) * n + (t >= 1 && t <= 7 ? 4 : 0) + (t >= 15);
}
constexpr int nreads(int t) {
  int n = 0;
  for (int i = 0; i < 3; i++) { const int I = 3 * t + i; n += t < 0 || I >= 59 ? 0 : I >= 43 ? 3 : 1; }
  return n;
}

template <bool HI>
DI void stage2(LAS char* sm, u32 lb, const Ctx& c, char* ws, int w, int lane, int ti2, int cj, const u16* x, u16* out) {
  const int tg = w & 1, cg = (w >> 1) & 1;
  constexpr int kh = HI;
  const int r = lane & 31, h = lane >> 5, g16 = (lane >> 4) & 1, q4 = (lane & 15) >> 2, pp = lane & 3;
  const u32 vlin = lane * 16;
  const u32 vw = (lane >> 3) * 1024 + 16 * ((lane & 7) ^ (((lane >> 4) & 1) << 2));
  const u32 wa0 = lb + (4 * h + q4) * 128 + ((cg ^ (q4 >> 1)) << 6) + 32 * g16 + 8 * pp, wa = wa0 + kh * 2048;
  u32x2 wf[43][2];
  sfor<0, R1F>([&](auto i) {
    constexpr int I = decltype(i)::value, n = 4 * I + 2 * kh, O0 = piece_l(n), O1 = piece_l(n + 1);
    trd<(O0 & 0xffff)>(wf[I][0], wa0 + (O0 & ~0xffff)); trd<(O1 & 0xffff)>(wf[I][1], wa0 + (O1 & ~0xffff));
  });
  lgwait();
  if constexpr (HI) vmwait<0>();
  bar();
  STAMP(5);
  u32 vq[4];
#pragma unroll
  for (int j = 0; j < 4; j++) vq[j] = vw + j * 7168;
  const char* so = c.wo + w * 65536 + cj * 128;
  if (HI && w == 7) st8d(ws + WS_F1 + (c.ti * 16 + c.jc) * 64 + 8, 0u, c.tag);
  if (!HI && w == 0) poll8(ws + WS_F1 + (ti2 >> 1) * 1024, (lane & 15) * 64, c.tag);
  else if (w < 6) dma_grp(c.wdc + (148 + 4 * w) * 8192, lb + WO_L + (w - 1) * 8192 + 4096, vq);
  dma_grp(so, lb + WO_L + w * 8192, vq);
  vmwait<4>();
  bar();
  STAMP(6);
  const u32 ooff = ((64 * ti2 + 32 * tg + r) * DM + 64 * cj + 32 * cg + 16 * kh + 8 * h) * 2;
  u32x4 xq;
  f32x16 accD, accS;
#pragma unroll
  for (int j = 0; j < 16; j++) { accD[j] = 0.f; accS[j] = 0.f; }
  {
    const int p0 = HI ? w + 4 : 2 * w;
    const char* hS = ws + WS_H + (size_t)((2 * ti2 + (p0 >= 6)) * NKS + (p0 >= 6 ? p0 - 6 : p0)) * 1024;
    const u32 mh = __builtin_amdgcn_readfirstlane(lb + p0 * 1024);
    u32 vh = vlin - 6144;
    auto issue_a = [&](auto st_) { constexpr int st = decltype(st_)::value; if constexpr (st <= 19) dma_ring<(st & 7) * SLOT2, 6144>(mh, vh, hS); };
    auto issue_b = [&](auto st_) { constexpr int st = decltype(st_)::value; if constexpr (st <= 19 && !HI) dma_ring2(vh, hS); };
    sfor<0, 8>([&](auto t_) { issue_a(t_); issue_b(t_); });
    sfor<R1F, 43>([&](auto i) {
      constexpr int I = decltype(i)::value, O = (I - R1F) * 8192 + 4096;
      trd<O>(wf[I][0], wa + WO_L); trd<O + 1024>(wf[I][1], wa + WO_L);
    });
    u32 ha[2], woa = wa + WO_L;
    ha[0] = lb + tg * 6144 + HI * 1024 + vlin; ha[1] = ha[0] + 4 * SLOT2;
    u32x4 hf[3][3];
    u32x2 of[3][3][2], fo;
    asm volatile("" : "+v"(accD), "+v"(accS), "+v"(ha[0]), "+v"(ha[1]), "+v"(woa));
    auto mm = [&](auto p_, auto i_) {
      constexpr int p = decltype(p_)::value, i = decltype(i_)::value, I = 3 * p + i;
      if constexpr (p >= 0 && I < 43) amfma(accD, cat(wf[I][0], wf[I][1]), hf[p % 3][i]);
      else if constexpr (p >= 0 && I < 59) amfma(accS, cat(of[p % 3][i][0], of[p % 3][i][1]), hf[p % 3][i]);
    };
    sfor<0, 22>([&](auto t_) {
      constexpr int t = decltype(t_)::value;
      using P = IC<t - 2>;
      if constexpr (t != 1) lgw<nreads(t - 1)>();
      mm(P{}, IC<0>{});
      if constexpr (t == 8 && !HI) {
        if (w == 0) {
          vmwait<6>();
          asm volatile("" : "+v"(fo));
          if (!__all(fo[0] == c.tag[0] && fo[1] == c.tag[1])) poll8(ws + WS_F1 + (ti2 >> 1) * 1024 + 8, (lane & 15) * 64, c.tag);
        }
      }
      if constexpr (t <= 19) {
        vmwait<need_n(!HI, t)>();
        bar();
        if constexpr (t == 0) STAMP(7);
        if constexpr (t == 14) STAMP(9);
        sfor<0, 3>([&](auto i_) {
          constexpr int i = decltype(i_)::value, I = 3 * t + i;
          if constexpr (I < 59) rd128<(t & 3) * SLOT2 + i * 2048>(hf[t % 3][i], ha[(t >> 2) & 1]);
          if constexpr (I >= 43 && I < 59) { trd<(I - 43) * 4096>(of[t % 3][i][0], woa); trd<(I - 43) * 4096 + 1024>(of[t % 3][i][1], woa); }
        });
      }
      mm(P{}, IC<1>{});
      if constexpr (t == 0) dma_grp(so + 32768, lb + WO_L + w * 8192 + 4096, vq);
      if constexpr (t >= 2) issue_a(IC<t + 6>{});
      mm(P{}, IC<2>{});
      if constexpr (t >= 2) issue_b(IC<t + 6>{});
      if constexpr (t == 14) { u32x4 t4; asm volatile("global_load_dwordx4 %0, %1, %2" : "=v"(t4) : "v"(ooff), "s"(x) : "memory"); xq = t4; }
      if constexpr (t == 4 && !HI) ld8_w0(fo, ws + WS_F1 + (ti2 >> 1) * 1024 + 8, (lane & 15) * 64, w);
      if constexpr (t == 19 && !HI) {
        if (blockIdx.x == 0 && w == 1) {
          poll_all(ws + WS_F1, lane * 64, c.tag);
          st8d(ws + WS_EP, 0u, c.tag);
        }
      }
    });
  }
  asm volatile("s_nop 15" : "+v"(accD), "+v"(accS));
  STAMP(10);
  {
    constexpr int m = 8 * kh, o = 8 - m;
    LAS f32x4* pd = (LAS f32x4*)(sm + 4 * SLOT2 + (w ^ 4) * 4096 + lane * 16);
    pd[0] = (f32x4){accD[o], accD[o + 1], accD[o + 2], accD[o + 3]}; pd[64] = (f32x4){accD[o + 4], accD[o + 5], accD[o + 6], accD[o + 7]};
    pd[128] = (f32x4){accS[o], accS[o + 1], accS[o + 2], accS[o + 3]}; pd[192] = (f32x4){accS[o + 4], accS[o + 5], accS[o + 6], accS[o + 7]};
    lgwait();
    bar();
    LAS f32x4* ps = (LAS f32x4*)(sm + 4 * SLOT2 + w * 4096 + lane * 16);
    vmwait<0>();
    asm volatile("" : "+v"(xq));
    u32 xr[4];
    { auto a = __builtin_amdgcn_permlane32_swap(xq[0], xq[2], false, false); xr[0] = a[0]; xr[2] = a[1];
      auto b = __builtin_amdgcn_permlane32_swap(xq[1], xq[3], false, false); xr[1] = b[0]; xr[3] = b[1]; }
    u32 ov[4];
#pragma unroll
    for (int i = 0; i < 2; i++) {
      const f32x4 d = ps[64 * i] + (f32x4){accD[m + 4 * i], accD[m + 4 * i + 1], accD[m + 4 * i + 2], accD[m + 4 * i + 3]};
      const f32x4 sv = ps[128 + 64 * i] + (f32x4){accS[m + 4 * i], accS[m + 4 * i + 1], accS[m + 4 * i + 2], accS[m + 4 * i + 3]};
#pragma unroll
      for (int j = 0; j < 2; j++) {
        const f32x2 dd = {d[2 * j], d[2 * j + 1]}, z = dd * sp2(-1.4426950408889634f);
        const f32x2 e = (f32x2){fexp2(z[0]), fexp2(z[1])} + sp2(1.f);
        const f32x2 g = dd * (f32x2){frcp(e[0]), frcp(e[1])};
        ov[2 * i + j] = pkv(pkfma((f32x2){sv[2 * j], sv[2 * j + 1]}, g, (f32x2){lo_f(xr[2 * i + j]), hi_f(xr[2 * i + j])}));
      }
    }
    auto a = __builtin_amdgcn_permlane32_swap(ov[0], ov[2], false, false);
    auto b = __builtin_amdgcn_permlane32_swap(ov[1], ov[3], false, false);
    *(u32x4*)((char*)out + ooff) = (u32x4){a[0], b[0], a[1], b[1]};
  }
  if constexpr (HI) asm volatile(".Lfused_end1:"); else asm volatile(".Lfused_end0:");
  STAMP(11);
}

__global__ __launch_bounds__(512, 2) void fused(const u16* __restrict__ x, const u16* __restrict__ wqkv, const u16* __restrict__ wphi,
                                                const u16* __restrict__ wo, const u16* __restrict__ wg, const u16* __restrict__ wu,
                                                const u16* __restrict__ wd, char* __restrict__ ws, u16* __restrict__ out) {
  __shared__ __attribute__((aligned(256))) char smem[LDSB];
  LAS char* sm = (LAS char*)smem;
  const u32 lb = (u32)(size_t)sm;
  const int b = blockIdx.x, xcd = b & 7, loc = b >> 3;
  const int w = __builtin_amdgcn_readfirstlane(threadIdx.x >> 6), lane = threadIdx.x & 63;
  asm volatile("" ::"s"(x), "s"(wqkv), "s"(wphi), "s"(wo), "s"(wg), "s"(wu), "s"(wd), "s"(ws), "s"(out));
  STAMP(0);
  Ctx c;
  c.ti = 4 * (xcd >> 1) + (loc & 3); c.jc = 8 * (xcd & 1) + (loc >> 2);
  const int ti2 = 4 * xcd + (loc >> 3), cj = loc & 7;
  c.xt = (const char*)x + c.ti * (128 * 1024);
  {
    const int rr = lane >> 3, pos = lane & 7, f = lane >> 4;
    const u32 va = rr * 1024 + 16 * (pos ^ f);
    set_m0<XR>(lb + w * 2048);
    const char* sx = c.xt + w * 16384;
    dma_p<0>(va, sx); dma_p<1>((va ^ 64) + 7168, sx);
    set_m0<P0R>(lb + w * 1024);
    dma_p<0>((lane >> 2) * 3072 + (lane & 3) * 16, (const char*)wqkv + 1024 + (w >> 2) * 1024 + c.jc * 64 + (w & 3) * 49152);
  }
  {
    u64 pc; u32 len, len1;
    asm volatile("s_getpc_b64 %0\n.Lfused_pc:\n\ts_mov_b32 %1, .Lfused_end0-.Lfused_pc\n\ts_mov_b32 %2, .Lfused_end1-.Lfused_pc" : "=s"(pc), "=s"(len), "=s"(len1));
    c.code = (const char*)(pc & ~15ull); c.clim = (max(len, len1) & ~15u) - 16u;
  }
  if (w < 2) dma16(c.code, min((u32)(loc * 1024 + lane * 16 + w * 32768), c.clim), lb + JUNK);
  c.wdc = (const char*)wd + cj * 128;
  c.x = (const char*)x; c.wqkv = (const char*)wqkv; c.wphi = (const char*)wphi; c.wg = (const char*)wg; c.wu = (const char*)wu; c.wd = (const char*)wd; c.wo = (const char*)wo;
  c.np = c.jc < 6 ? 6 : 5;
  c.p0 = c.jc < 6 ? 6 * c.jc : 6 + 5 * c.jc;
  if (w >= 6) stage1<2, 3>(sm, lb, c, ws, w, lane);
  else if (w >= 4) stage1<1, 3>(sm, lb, c, ws, w, lane);
  else if (c.np == 6) stage1<0, 4>(sm, lb, c, ws, w, lane);
  else stage1<0, 3>(sm, lb, c, ws, w, lane);
  if (w >= 4) stage2<true>(sm, lb, c, ws, w, lane, ti2, cj, x, out);
  else stage2<false>(sm, lb, c, ws, w, lane, ti2, cj, x, out);
}

extern "C" void kernel_launch(void* const* d_in, const int* in_sizes, int n_in, void* d_out, int out_size, void* d_ws,
                              size_t ws_size, hipStream_t stream) {
  fused<<<256, 512, 0, stream>>>((const u16*)d_in[0], (const u16*)d_in[1], (const u16*)d_in[2], (const u16*)d_in[3],
                                 (const u16*)d_in[5], (const u16*)d_in[6], (const u16*)d_in[7], (char*)d_ws, (u16*)d_out);
}
```
